```python
import torch

def _softmax(x, axis=-1):
    return torch.softmax(x, dim=axis)
def _gelu(x):
    return torch.nn.functional.gelu(x, approximate="tanh")
def _silu(x):
    return torch.nn.functional.silu(x)
def _sigmoid(x):
    return torch.sigmoid(x)
def _layernorm(x, axis=-1, eps=1e-5):
    m = x.mean(dim=axis, keepdim=True)
    v = x.var(dim=axis, keepdim=True, unbiased=False)
    return (x - m) / torch.sqrt(v + eps)
def _scatter_add(tgt, idx, src, axis=0):
    return tgt.clone().index_add_(axis, idx, src)
def _segment_sum(src, seg, n):
    out = torch.zeros((n,) + src.shape[1:], dtype=src.dtype)
    return out.index_add_(0, seg, src)
def _pad(x, pw):
    flat = tuple(v for pair in reversed(pw) for v in pair)
    return torch.nn.functional.pad(x, flat)

kernel_name = 'standalone__gqa_block_cached__near_ridge'

def setup_inputs():

    _w = (lambda a: (lambda t: t.to(torch.bfloat16) if t.is_floating_point() else t)(torch.as_tensor(a)))
    B, Sq, D = 16, 80, 576
    Hq, Hkv, Sk, Dh = 32, 16, 1024, 128
    assert Hq % Hkv == 0, (Hq, Hkv)
    return {
        "x": _w((lambda *s: torch.randn(*s, dtype=torch.bfloat16) * 0.8)(B, Sq, D)),
        "k_cache": _w((lambda *s: torch.randn(*s, dtype=torch.bfloat16) * 0.8)(B, Hkv, Sk, Dh)),
        "v_cache": _w((lambda *s: torch.randn(*s, dtype=torch.bfloat16) * 0.05)(B, Hkv, Sk, Dh)),
        "wq": _w((lambda *s: torch.randn(*s, dtype=torch.bfloat16) * 0.8)(D, Hq * Dh) * (D ** -0.5)),
        "wo": _w((lambda *s: torch.randn(*s, dtype=torch.bfloat16) * 0.05)(Hq * Dh, D) * ((Hq * Dh) ** -0.5)),
    }

def reference(x, k_cache, v_cache, wq, wo):
    B, Sq, D = x.shape
    Hq, Dh = 32, 128
    Hkv = k_cache.shape[1]
    rep = Hq // Hkv
    q = (lambda x, *d: x.permute(*d))((x @ wq).reshape(B, Sq, Hq, Dh), 0, 2, 1, 3)
    k = (lambda x, n, axis: torch.repeat_interleave(x, n, dim=axis))(k_cache, rep, axis=1)
    v = (lambda x, n, axis: torch.repeat_interleave(x, n, dim=axis))(v_cache, rep, axis=1)
    scores = torch.einsum("bhqd,bhkd->bhqk", q, k) * (Dh ** -0.5)
    p = _softmax(scores, axis=-1)
    o = torch.einsum("bhqk,bhkd->bhqd", p, v)
    return (lambda x, *d: x.permute(*d))(o, 0, 2, 1, 3).reshape(B, Sq, Hq * Dh) @ wo
```

```cpp
#include <hip/hip_runtime.h>
#include <hip/hip_bf16.h>

typedef __bf16 bf16x8 __attribute__((ext_vector_type(8)));
typedef __bf16 bf16x4 __attribute__((ext_vector_type(4)));
typedef __bf16 bf16x2 __attribute__((ext_vector_type(2)));
typedef float f32x4 __attribute__((ext_vector_type(4)));
typedef float f32x2 __attribute__((ext_vector_type(2)));
typedef unsigned u32x4 __attribute__((ext_vector_type(4)));
typedef unsigned u32x2 __attribute__((ext_vector_type(2)));
#define LAS __attribute__((address_space(3)))
#define DI __device__ __forceinline__
#define GAP() __builtin_amdgcn_sched_barrier(0)
#define MFMA(a, b, c) __builtin_amdgcn_mfma_f32_16x16x32_bf16((a), (b), (c), 0, 0, 0)

#define KV_NT 1

constexpr unsigned KRING = 0, VRING = 49152, WRING = 98304, WSLOT = 21504, XOFF = 16384;
constexpr unsigned QAREA = WRING;
constexpr unsigned SPARE = WRING + 40960;
constexpr unsigned LDS_BYTES = 163840;
constexpr int PART_BYTES = 92160;

DI unsigned rfl(unsigned x) { return __builtin_amdgcn_readfirstlane(x); }
DI unsigned cvtpk(float lo, float hi) { f32x2 v = {lo, hi}; bf16x2 b = __builtin_convertvector(v, bf16x2); return __builtin_bit_cast(unsigned, b); }
DI bf16x8 ldsr128(unsigned a) { return *(const LAS bf16x8*)a; }
DI bf16x4 ldstr(unsigned a) { return __builtin_amdgcn_ds_read_tr16_b64_v4bf16((LAS bf16x4*)a); }
DI bf16x8 cat(bf16x4 a, bf16x4 b) { return __builtin_shufflevector(a, b, 0, 1, 2, 3, 4, 5, 6, 7); }
DI void barrier() { asm volatile("s_barrier" ::: "memory"); }
template <int N> DI void wait_vm() { asm volatile("s_waitcnt vmcnt(%0)" :: "i"(N) : "memory"); }
template <bool NTP> DI void dma(unsigned lds, unsigned voff, __amdgpu_buffer_rsrc_t rs, unsigned soff) {
  if constexpr (NTP) asm volatile("s_mov_b32 m0, %0\n\ts_nop 0\n\tbuffer_load_dwordx4 %1, %2, %3 offen nt lds" :: "s"(lds), "v"(voff), "s"(rs), "s"(soff) : "memory");
  else               asm volatile("s_mov_b32 m0, %0\n\ts_nop 0\n\tbuffer_load_dwordx4 %1, %2, %3 offen lds" :: "s"(lds), "v"(voff), "s"(rs), "s"(soff) : "memory");
}
DI unsigned lane_id() { unsigned l; asm volatile("v_mbcnt_lo_u32_b32 %0, -1, 0\n\tv_mbcnt_hi_u32_b32 %0, -1, %0" : "=v"(l)); return l; }
DI unsigned swk(unsigned r4) { return (4u - r4) & 3u; }

constexpr int hu_cum(int i) { return i < 7 ? 0 : (i - 6 > 11 ? 11 : i - 6); }
constexpr int hu_at(int i) { return i < 0 ? 0 : hu_cum(i) - (i > 0 ? hu_cum(i - 1) : 0); }
constexpr unsigned blk_addr(int idx) {
  return idx == 0 ? SPARE : idx == 1 ? SPARE + 8192
       : idx == 2 ? KRING + (26 % 6) * 8192 : idx == 3 ? KRING + (27 % 6) * 8192
       : (idx & 1) == 0 ? KRING + ((28 + (idx - 4) / 2) % 6) * 8192 : VRING + ((26 + (idx - 5) / 2) % 6) * 8192;
}
constexpr int n_pre(int t) { return (t >= 0 && t < 24 ? 2 : 0) + (t == 24 || t == 25 ? 2 : 0); }
constexpr int n_v(int t) { return (t >= 0 && t + 5 <= 31) ? 2 : 0; }
constexpr int n_post(int t) { return (t + 7 <= 31 ? 2 : 0) + (t >= 26 ? 4 : 0); }
constexpr int n_all(int t) { return n_pre(t) + n_v(t) + n_post(t); }
constexpr int wait_b(int t) {
  int n = 0;
  if (t + 1 <= 4) { n = 2 * (10 - (2 * (t + 1) + 2)); for (int u = -1; u < t; ++u) n += n_all(u); }
  else { n = n_post(t - 4); for (int u = t - 3; u < t; ++u) n += n_all(u); }
  return n;
}

#define STAMP(i) do { } while (0)

template <int I> struct IC { static constexpr int v = I; };
template <int N, int I = 0, class F> DI void sfor(F&& f) { if constexpr (I < N) { f(IC<I>{}); sfor<N, I + 1>(f); } }

template <int NT> DI void compute_wave(const unsigned w, const unsigned lane, char* part, unsigned long long* stamps) {
  const unsigned r = lane & 15, g = lane >> 4, q4 = (lane >> 2) & 3, p4 = lane & 3;
  {
    f32x4 acc[4][5];
#pragma unroll
    for (int t = 0; t < 4; ++t)
#pragma unroll
      for (int pt = 0; pt < 5; ++pt) acc[t][pt] = (f32x4){0.f, 0.f, 0.f, 0.f};
    const unsigned wa0 = WRING + w * 4096 + g * 1024 + q4 * 64 + 32 * (g & 1) + 8 * p4;
    const unsigned wa1 = WRING + w * 4096 + g * 1024 + q4 * 64 + 32 * ((g & 1) ^ 1) + 8 * p4;
    const unsigned xa = WRING + XOFF + r * 64 + 16 * (g ^ swk(r >> 2));
#pragma unroll 1
    for (int i3 = 0; i3 < 6; ++i3) {
#pragma unroll
      for (int s = 0; s < 3; ++s) {
        barrier();
        bf16x8 a[4], x[5];
#pragma unroll
        for (int t = 0; t < 4; ++t) { const unsigned b = ((t & 1) ? wa1 : wa0) + s * WSLOT + (t >> 1) * 512; a[t] = cat(ldstr(b), ldstr(b + 256)); }
#pragma unroll
        for (int pt = 0; pt < 5; ++pt) x[pt] = ldsr128(xa + s * WSLOT + pt * 1024);
#pragma unroll
        for (int pt = 0; pt < 5; ++pt)
#pragma unroll
          for (int t = 0; t < 4; ++t) acc[t][pt] = MFMA(a[t], x[pt], acc[t][pt]);
      }
    }
    STAMP(1);
    const float c = 0.12751743082459868f;
    const unsigned h = w >> 1, dh = w & 1;
#pragma unroll
    for (int pt = 0; pt < 5; ++pt)
#pragma unroll
      for (int t = 0; t < 4; ++t) {
        u32x2 v = {cvtpk(acc[t][pt][0] * c, acc[t][pt][1] * c), cvtpk(acc[t][pt][2] * c, acc[t][pt][3] * c)};
        const unsigned a = QAREA + ((h * 5 + pt) * 4 + 2 * dh + (t >> 1)) * 1024 + (16 * (2 * (t & 1) + (g >> 1)) + r) * 16 + 8 * (g & 1);
        *(LAS u32x2*)a = v;
      }
    asm volatile("s_waitcnt lgkmcnt(0)" ::: "memory");
  }
  barrier();
  const unsigned T0 = NT == 3 ? w * 5 : (w - 2) * 5 + 3;
  bf16x8 Q[NT][4];
#pragma unroll
  for (int T = 0; T < NT; ++T)
#pragma unroll
    for (int ks = 0; ks < 4; ++ks) Q[T][ks] = ldsr128(QAREA + ((T0 + T) * 4 + ks) * 1024 + lane * 16);
  const unsigned ka = KRING + (r >> 3) * 2048 + (r & 7) * 64 + 16 * (g ^ swk(r >> 2));
  const unsigned va0 = VRING + (g >> 1) * 2048 + (4 * (g & 1) + q4) * 64 + 32 * (g & 1) + 8 * p4;
  const unsigned va1 = VRING + (g >> 1) * 2048 + (4 * (g & 1) + q4) * 64 + 32 * ((g & 1) ^ 1) + 8 * p4;
  f32x4 O[8][NT], L[NT], NM[NT], S[2][NT];
  u32x4 P[NT];
  bf16x8 Kf[4][2];
  const __bf16 one = (__bf16)1.0f;
  bf16x8 ones = {one, one, one, one, one, one, one, one};
  asm volatile("" : "+v"(ones));
#pragma unroll
  for (int T = 0; T < NT; ++T) {
    L[T] = (f32x4){0.f, 0.f, 0.f, 0.f};
#pragma unroll
    for (int db = 0; db < 8; ++db) O[db][T] = (f32x4){0.f, 0.f, 0.f, 0.f};
  }
  {
#pragma unroll
    for (int ks = 0; ks < 4; ++ks)
#pragma unroll
      for (int kt = 0; kt < 2; ++kt) Kf[ks][kt] = ldsr128(ka + kt * 4096 + ks * 512);
#pragma unroll
    for (int T = 0; T < NT; ++T)
#pragma unroll
      for (int kt = 0; kt < 2; ++kt) {
        f32x4 s = {0.f, 0.f, 0.f, 0.f};
#pragma unroll
        for (int ks = 0; ks < 4; ++ks) s = MFMA(Kf[ks][kt], Q[T][ks], s);
        S[kt][T] = s;
      }
    barrier();
#pragma unroll
    for (int ks = 0; ks < 2; ++ks)
#pragma unroll
      for (int kt = 0; kt < 2; ++kt) Kf[ks][kt] = ldsr128(ka + 8192 + kt * 4096 + ks * 512);
#pragma unroll
    for (int T = 0; T < NT; ++T) {
      float m = fmaxf(fmaxf(fmaxf(S[0][T][0], S[0][T][1]), fmaxf(S[0][T][2], S[0][T][3])), fmaxf(fmaxf(S[1][T][0], S[1][T][1]), fmaxf(S[1][T][2], S[1][T][3])));
      m = fmaxf(m, __shfl_xor(m, 16)); m = fmaxf(m, __shfl_xor(m, 32));
      NM[T] = (f32x4){-m, -m, -m, -m};
      asm volatile("" : "+v"(NM[T]));
#pragma unroll
      for (int kt = 0; kt < 2; ++kt)
#pragma unroll
        for (int i = 0; i < 4; ++i) S[kt][T][i] = __builtin_amdgcn_exp2f(S[kt][T][i] - m);
      P[T] = (u32x4){cvtpk(S[0][T][0], S[0][T][1]), cvtpk(S[0][T][2], S[0][T][3]), cvtpk(S[1][T][0], S[1][T][1]), cvtpk(S[1][T][2], S[1][T][3])};
    }
  }
  STAMP(2);
  unsigned ko = 8192, vo = 0;
  GAP();
#pragma unroll 1
  for (int t = 0; t < 31; ++t) {
    const unsigned kb = ka + ko, vb0 = va0 + vo, vb1 = va1 + vo;
    ko = ko == 5 * 8192 ? 0 : ko + 8192; vo = vo == 5 * 8192 ? 0 : vo + 8192;
    const unsigned kn = ka + ko;
    bf16x4 Vl[8], Vh[8];
    constexpr int NQ = 8 * NT, NE = 8 * NT;
    sfor<NQ>([&](auto mc) {
      constexpr int m = decltype(mc)::v, ks = m / (2 * NT), T = (m % (2 * NT)) / 2, kt = m & 1;
      S[kt][T] = MFMA(Kf[ks][kt], Q[T][ks], ks == 0 ? NM[T] : S[kt][T]);
      if constexpr (m < 4) Kf[2 + (m >> 1)][m & 1] = ldsr128(kb + (m & 1) * 4096 + (2 + (m >> 1)) * 512);
      constexpr int lo = m < 4 ? 0 : (16 * (m - 4)) / (NQ - 4), hi = m < 4 ? 0 : (16 * (m - 3)) / (NQ - 4);
      sfor<hi - lo>([&](auto ec) { constexpr int e = lo + decltype(ec)::v, db = e >> 1;
        if constexpr (e & 1) Vh[db] = ldstr(((db & 1) ? vb1 : vb0) + 4096 + (db >> 1) * 512); else Vl[db] = ldstr(((db & 1) ? vb1 : vb0) + (db >> 1) * 512); });
      GAP();
    });
    constexpr int NP = 9 * NT;
    sfor<NP>([&](auto mc) {
      constexpr int m = decltype(mc)::v, T = m % NT, db = m / NT - 1;
      if constexpr (db < 0) L[T] = MFMA(ones, __builtin_bit_cast(bf16x8, P[T]), L[T]);
      else O[db][T] = MFMA(cat(Vl[db], Vh[db]), __builtin_bit_cast(bf16x8, P[T]), O[db][T]);
      if constexpr (m < NE) { constexpr int eT = m / 8, ekt = (m >> 2) & 1, ei = m & 3; S[ekt][eT][ei] = __builtin_amdgcn_exp2f(S[ekt][eT][ei]); }
      if constexpr (m == 5 * NT) barrier();
      if constexpr (m > 5 * NT && m <= 5 * NT + 4) { constexpr int e = m - 5 * NT - 1; Kf[e >> 1][e & 1] = ldsr128(kn + (e & 1) * 4096 + (e >> 1) * 512); }
      if constexpr (db == 7) P[T] = (u32x4){cvtpk(S[0][T][0], S[0][T][1]), cvtpk(S[0][T][2], S[0][T][3]), cvtpk(S[1][T][0], S[1][T][1]), cvtpk(S[1][T][2], S[1][T][3])};
      GAP();
    });
  }
  const unsigned lane2 = lane_id();
  { const unsigned lane = lane2; STAMP(3); }
  {
    const unsigned lane = lane2;
    const unsigned vb0 = va0 + vo, vb1 = va1 + vo;
    bf16x8 Vf[8];
#pragma unroll
    for (int db = 0; db < 8; ++db) { const unsigned b = ((db & 1) ? vb1 : vb0) + (db >> 1) * 512; Vf[db] = cat(ldstr(b), ldstr(b + 4096)); }
#pragma unroll
    for (int T = 0; T < NT; ++T) L[T] = MFMA(ones, __builtin_bit_cast(bf16x8, P[T]), L[T]);
#pragma unroll
    for (int db = 0; db < 8; ++db)
#pragma unroll
      for (int T = 0; T < NT; ++T) O[db][T] = MFMA(Vf[db], __builtin_bit_cast(bf16x8, P[T]), O[db][T]);
#pragma unroll
    for (int T = 0; T < NT; ++T) {
      const float il = 1.0f / L[T][0];
#pragma unroll
      for (int s = 0; s < 4; ++s) {
        const f32x4 a = O[2 * s][T], b = O[2 * s + 1][T];
        u32x4 v = {cvtpk(a[0] * il, a[1] * il), cvtpk(a[2] * il, a[3] * il), cvtpk(b[0] * il, b[1] * il), cvtpk(b[2] * il, b[3] * il)};
        *(LAS u32x4*)(QAREA + ((T0 + T) * 4 + s) * 1024 + lane * 16) = v;
      }
    }
    asm volatile("s_waitcnt lgkmcnt(0)" ::: "memory");
  }
  barrier();
  {
    const unsigned lane = lane2, g = lane >> 4, q4 = (lane >> 2) & 3, p4 = lane & 3;
    STAMP(4);
    unsigned wb[3][2];
#pragma unroll
    for (int i = 0; i < 3; ++i) {
      const unsigned tau = 3 * w + i, pp = tau >> 1, u = tau & 1;
      unsigned b0 = blk_addr(0), b1 = blk_addr(6);
#pragma unroll
      for (int k = 1; k < 6; ++k) { b0 = pp == (unsigned)k ? blk_addr(k) : b0; b1 = pp == (unsigned)k ? blk_addr(6 + k) : b1; }
      const unsigned la = (4 * g + q4) * 64 + 32 * (u ^ (g & 1)) + 8 * p4;
      wb[i][0] = b0 + la; wb[i][1] = b1 + la;
    }
    f32x4 acc[3][5];
#pragma unroll
    for (int i = 0; i < 3; ++i)
#pragma unroll
      for (int pt = 0; pt < 5; ++pt) acc[i][pt] = (f32x4){0.f, 0.f, 0.f, 0.f};
#pragma unroll
    for (int s = 0; s < 8; ++s) {
      bf16x8 a[3], o[5];
#pragma unroll
      for (int i = 0; i < 3; ++i) { const unsigned b = wb[i][s >> 2] + (s & 3) * 2048; a[i] = cat(ldstr(b), ldstr(b + 1024)); }
#pragma unroll
      for (int pt = 0; pt < 5; ++pt) o[pt] = ldsr128(QAREA + (((s >> 2) * 5 + pt) * 4 + (s & 3)) * 1024 + lane * 16);
#pragma unroll
      for (int pt = 0; pt < 5; ++pt)
#pragma unroll
        for (int i = 0; i < 3; ++i) acc[i][pt] = MFMA(a[i], o[pt], acc[i][pt]);
    }
    const __amdgpu_buffer_rsrc_t rp = __builtin_amdgcn_make_buffer_rsrc(part, 0, PART_BYTES, 0x00020000);
    const unsigned fb = (60 + w * 7) * 1024;
#pragma unroll
    for (int pt = 0; pt < 5; ++pt) {
      u32x4 v = {cvtpk(acc[0][pt][0], acc[0][pt][1]), cvtpk(acc[0][pt][2], acc[0][pt][3]), cvtpk(acc[1][pt][0], acc[1][pt][1]), cvtpk(acc[1][pt][2], acc[1][pt][3])};
      __builtin_amdgcn_raw_buffer_store_b128(v, rp, lane * 16, fb + pt * 1024, 16);
    }
#pragma unroll
    for (int f = 0; f < 2; ++f) {
      u32x4 v = {cvtpk(acc[2][2 * f][0], acc[2][2 * f][1]), cvtpk(acc[2][2 * f][2], acc[2][2 * f][3]), cvtpk(acc[2][2 * f + 1][0], acc[2][2 * f + 1][1]), cvtpk(acc[2][2 * f + 1][2], acc[2][2 * f + 1][3])};
      __builtin_amdgcn_raw_buffer_store_b128(v, rp, lane * 16, fb + (5 + f) * 1024, 16);
    }
    u32x2 v2 = {cvtpk(acc[2][4][0], acc[2][4][1]), cvtpk(acc[2][4][2], acc[2][4][3])};
    __builtin_amdgcn_raw_buffer_store_b64(v2, rp, lane * 8, 88 * 1024 + w * 512, 16);
  }
}

DI void helper_wave(const unsigned j, const unsigned lane, const unsigned b, const unsigned kvh, const char* x, const char* kc, const char* vc, const char* wq, const char* wo, char* part, unsigned long long* stamps) {
  const __amdgpu_buffer_rsrc_t rx = __builtin_amdgcn_make_buffer_rsrc((void*)(x + (size_t)b * 92160), 0, 92160, 0x00020000);
  const __amdgpu_buffer_rsrc_t rk = __builtin_amdgcn_make_buffer_rsrc((void*)(kc + (size_t)(b * 16 + kvh) * 262144), 0, 262144, 0x00020000);
  const __amdgpu_buffer_rsrc_t rv = __builtin_amdgcn_make_buffer_rsrc((void*)(vc + (size_t)(b * 16 + kvh) * 262144), 0, 262144, 0x00020000);
  const __amdgpu_buffer_rsrc_t rq = __builtin_amdgcn_make_buffer_rsrc((void*)(wq + (size_t)kvh * 512), 0, 576 * 8192 - kvh * 512, 0x00020000);
  const __amdgpu_buffer_rsrc_t ro = __builtin_amdgcn_make_buffer_rsrc((void*)(wo + (size_t)kvh * 256 * 1152), 0, 256 * 1152, 0x00020000);
  const unsigned cb = lane >> 5, row = (lane >> 2) & 7, c4 = lane & 3, r16 = lane >> 2;
  const unsigned vk = (8 * j + row) * 256 + cb * 64 + 16 * (c4 ^ swk(2 * (j & 1) + (row >> 2)));
  const unsigned vv = (8 * j + row) * 256 + cb * 64 + 16 * (c4 ^ (2 * (row >> 2)));
  const unsigned vq = (8 * j + row) * 8192 + cb * 64 + 16 * (c4 ^ (2 * (j & 1)));
  const unsigned vx = (16 * j + r16) * 1152 + 16 * (c4 ^ swk(r16 >> 2));
  const unsigned vx4 = (64 + 4 * j + (r16 & 3)) * 1152 + 16 * (c4 ^ swk(j));
  const unsigned vo = r16 * 1152 + 16 * (c4 ^ (2 * ((r16 >> 2) & 1)));
  auto dma_k = [&](int t) { dma<KV_NT>(KRING + (t % 6) * 8192 + j * 2048, vk, rk, t * 8192); dma<KV_NT>(KRING + (t % 6) * 8192 + j * 2048 + 1024, vk, rk, t * 8192 + 128); };
  auto dma_v = [&](int t) { dma<KV_NT>(VRING + (t % 6) * 8192 + j * 2048, vv, rv, t * 8192); dma<KV_NT>(VRING + (t % 6) * 8192 + j * 2048 + 1024, vv, rv, t * 8192 + 128); };
  unsigned ksn = (b * 9 / 8 + 9 * (kvh & 1)) % 18;
  auto dma_w = [&](int i) {
    const unsigned slot = WRING + (i % 3) * WSLOT;
    dma<false>(slot + XOFF + j * 1024, vx, rx, ksn * 64);
    if (lane < 16) dma<false>(slot + XOFF + 4096 + j * 256, vx4, rx, ksn * 64);
#pragma unroll
    for (int ns = 0; ns < 4; ++ns) dma<false>(slot + (ns * 4) * 1024 + j * 1024, vq, rq, ksn * 262144 + ns * 128);
    ksn = ksn == 17 ? 0 : ksn + 1;
  };
  auto dma_hu = [&](int pos) { if (pos == 0) dma_k(0); else if (pos & 1) dma_k((pos + 1) / 2); else dma_v(pos / 2 - 1); };
  asm volatile("s_nop 4" ::: "memory");
  dma_w(0); dma_w(1); dma_w(2);
  sfor<18>([&](auto ic) {
    constexpr int i = decltype(ic)::v;
    constexpr int n = i == 0 ? 12 : i == 1 ? 6 + 2 * hu_at(0) : i == 2 ? 2 * hu_at(0) + 6 + 2 * hu_at(1) : 2 * hu_at(i - 2) + (i + 1 < 18 ? 6 : 0) + 2 * hu_at(i - 1);
    wait_vm<n>();
    barrier();
    if constexpr (i >= 1 && i + 2 < 18) dma_w(i + 2);
    sfor<hu_at(i)>([&](auto ec) { dma_hu(hu_cum(i) - hu_at(i) + decltype(ec)::v); });
  });
  wait_vm<20>();
  barrier();
  bf16x8 W[3][2][8];
  const unsigned g = lane >> 4, q4 = (lane >> 2) & 3, p4 = lane & 3;
  const unsigned sa0 = QAREA + j * 10240 + (4 * g + q4) * 64 + 32 * (g & 1) + 8 * p4, sa1 = QAREA + j * 10240 + (4 * g + q4) * 64 + 32 * ((g & 1) ^ 1) + 8 * p4;
  auto dma_blk = [&](int idx) {
    const unsigned so = ((idx / 6) * 128 + 32 * j) * 1152 + (384 + 32 * (idx % 6)) * 2;
    dma<false>(blk_addr(idx) + j * 2048, vo, ro, so); dma<false>(blk_addr(idx) + j * 2048 + 1024, vo, ro, so + 16 * 1152);
  };
  sfor<32>([&](auto tc) {
    constexpr int t = decltype(tc)::v - 1;
    wait_vm<wait_b(t)>();
    barrier();
    if constexpr (t >= 0 && t < 24) {
      asm volatile("s_waitcnt lgkmcnt(0)" ::: "memory");
      const unsigned so = (32 * (t % 8)) * 1152 + (96 * j + 32 * (t / 8)) * 2;
      dma<false>(QAREA + j * 10240 + (t % 5) * 2048, vo, ro, so); dma<false>(QAREA + j * 10240 + (t % 5) * 2048 + 1024, vo, ro, so + 16 * 1152);
    }
    if constexpr (t == 24 || t == 25) dma_blk(t - 24);
    if constexpr (n_v(t) > 0) dma_v(t + 5);
    if constexpr (t + 7 <= 31) dma_k(t + 7);
    if constexpr (t >= 26) { dma_blk(2 * (t - 25)); dma_blk(2 * (t - 25) + 1); }
    if constexpr (t >= 4 && t < 28) {
      constexpr int u = t - 4;
      W[u / 8][0][u % 8] = cat(ldstr(sa0 + (u % 5) * 2048), ldstr(sa0 + (u % 5) * 2048 + 1024));
      W[u / 8][1][u % 8] = cat(ldstr(sa1 + (u % 5) * 2048), ldstr(sa1 + (u % 5) * 2048 + 1024));
      asm volatile("" : "+v"(W[u / 8][0][u % 8]), "+v"(W[u / 8][1][u % 8]));
    }
  });
  wait_vm<0>();
  barrier();
  const __amdgpu_buffer_rsrc_t rp = __builtin_amdgcn_make_buffer_rsrc(part, 0, PART_BYTES, 0x00020000);
#pragma unroll
  for (int p = 0; p < 3; ++p) {
    f32x4 acc[2][5];
#pragma unroll
    for (int pt = 0; pt < 5; ++pt) { acc[0][pt] = (f32x4){0.f, 0.f, 0.f, 0.f}; acc[1][pt] = (f32x4){0.f, 0.f, 0.f, 0.f}; }
#pragma unroll
    for (int s = 0; s < 8; ++s)
#pragma unroll
      for (int pt = 0; pt < 5; ++pt) {
        const bf16x8 o = ldsr128(QAREA + (((s >> 2) * 5 + pt) * 4 + (s & 3)) * 1024 + lane * 16);
        acc[0][pt] = MFMA(W[p][0][s], o, acc[0][pt]); acc[1][pt] = MFMA(W[p][1][s], o, acc[1][pt]);
      }
#pragma unroll
    for (int pt = 0; pt < 5; ++pt) {
      u32x4 v = {cvtpk(acc[0][pt][0], acc[0][pt][1]), cvtpk(acc[0][pt][2], acc[0][pt][3]), cvtpk(acc[1][pt][0], acc[1][pt][1]), cvtpk(acc[1][pt][2], acc[1][pt][3])};
      __builtin_amdgcn_raw_buffer_store_b128(v, rp, lane * 16, ((j * 3 + p) * 5 + pt) * 1024, 16);
    }
    asm volatile("" ::: "memory"); GAP();
  }
}

__global__ __launch_bounds__(512, 2) void gqa_main(const char* __restrict__ x, const char* __restrict__ kc, const char* __restrict__ vc, const char* __restrict__ wq, const char* __restrict__ wo, char* __restrict__ ws) {
  extern __shared__ __attribute__((aligned(16))) char smem[];
  const unsigned lane = threadIdx.x & 63, w = rfl(threadIdx.x >> 6);
  const unsigned wg = blockIdx.x, kvh = 2 * (wg & 7) + ((wg >> 3) & 1), b = wg >> 4;
  char* part = ws + (size_t)(b * 16 + kvh) * PART_BYTES;
  unsigned long long* stamps = nullptr; unsigned long long* cst = nullptr;
  if (w >= 4) helper_wave(w - 4, lane, b, kvh, x, kc, vc, wq, wo, part, stamps);
  else if (w < 2) compute_wave<3>(w, lane, part, cst);
  else compute_wave<2>(w, lane, part, cst);
}

__global__ __launch_bounds__(384) void gqa_reduce(const char* __restrict__ ws, unsigned short* __restrict__ out) {
  const unsigned wg = blockIdx.x, tid = threadIdx.x, b = wg >> 4, c = wg & 15;
  if (tid >= 360) return;
  const unsigned G = 360 * c + tid;
  const u32x4* src = (const u32x4*)(ws + (size_t)b * 16 * PART_BYTES) + G;
  u32x4 v[16];
#pragma unroll
  for (int k = 0; k < 16; ++k) v[k] = src[k * (PART_BYTES / 16)];
  float a[8] = {0.f, 0.f, 0.f, 0.f, 0.f, 0.f, 0.f, 0.f};
#pragma unroll
  for (int k = 0; k < 16; ++k)
#pragma unroll
    for (int e = 0; e < 4; ++e) { a[2 * e] += __uint_as_float(v[k][e] << 16); a[2 * e + 1] += __uint_as_float(v[k][e] & 0xffff0000u); }
  const unsigned F = G >> 6, l = G & 63, g = l >> 4, r = l & 15;
  unsigned n0, n1, pos0, pos1;
  if (F < 60) { n0 = 32 * (F / 5) + 4 * g; n1 = n0 + 16; pos0 = pos1 = 16 * (F % 5) + r; }
  else if (F < 88) { const unsigned w = (F - 60) / 7, f = (F - 60) % 7;
    if (f < 5) { n0 = 384 + 48 * w + 4 * g; n1 = n0 + 16; pos0 = pos1 = 16 * f + r; }
    else { n0 = n1 = 384 + 48 * w + 32 + 4 * g; pos0 = 32 * (f - 5) + r; pos1 = pos0 + 16; } }
  else { const unsigned G2 = G - 88 * 64, w = G2 >> 5, l0 = 2 * (G2 & 31); n0 = n1 = 384 + 48 * w + 32 + 4 * (l0 >> 4); pos0 = 64 + (l0 & 15); pos1 = pos0 + 1; }
  *(u32x2*)(out + (size_t)(b * 80 + pos0) * 576 + n0) = (u32x2){cvtpk(a[0], a[1]), cvtpk(a[2], a[3])};
  *(u32x2*)(out + (size_t)(b * 80 + pos1) * 576 + n1) = (u32x2){cvtpk(a[4], a[5]), cvtpk(a[6], a[7])};
}

extern "C" void kernel_launch(void* const* d_in, const int* in_sizes, int n_in, void* d_out, int out_size, void* d_ws, size_t ws_size, hipStream_t stream) {
  (void)hipFuncSetAttribute((const void*)gqa_main, hipFuncAttributeMaxDynamicSharedMemorySize, (int)LDS_BYTES);
  gqa_main<<<256, 512, LDS_BYTES, stream>>>((const char*)d_in[0], (const char*)d_in[1], (const char*)d_in[2], (const char*)d_in[3], (const char*)d_in[4], (char*)d_ws);
  gqa_reduce<<<256, 384, 0, stream>>>((const char*)d_ws, (unsigned short*)d_out);
}
```

```cpp
#include <hip/hip_runtime.h>
#include <hip/hip_bf16.h>

typedef __bf16 bf16x8 __attribute__((ext_vector_type(8)));
typedef __bf16 bf16x4 __attribute__((ext_vector_type(4)));
typedef __bf16 bf16x2 __attribute__((ext_vector_type(2)));
typedef float f32x4 __attribute__((ext_vector_type(4)));
typedef float f32x2 __attribute__((ext_vector_type(2)));
typedef unsigned u32x4 __attribute__((ext_vector_type(4)));
typedef unsigned u32x2 __attribute__((ext_vector_type(2)));
#define LAS __attribute__((address_space(3)))
#define DI __device__ __forceinline__
#define GAP() __builtin_amdgcn_sched_barrier(0)
#define MFMA(a, b, c) __builtin_amdgcn_mfma_f32_16x16x32_bf16((a), (b), (c), 0, 0, 0)

#define KV_NT 1

constexpr unsigned KRING = 0, VRING = 49152, WRING = 98304, WSLOT = 21504, XOFF = 16384;
constexpr unsigned QAREA = WRING;
constexpr unsigned SPARE = WRING + 40960;
constexpr unsigned LDS_BYTES = 163840;
constexpr int PART_BYTES = 92160;

DI unsigned rfl(unsigned x) { return __builtin_amdgcn_readfirstlane(x); }
DI unsigned cvtpk(float lo, float hi) { f32x2 v = {lo, hi}; bf16x2 b = __builtin_convertvector(v, bf16x2); return __builtin_bit_cast(unsigned, b); }
DI bf16x8 ldsr128(unsigned a) { return *(const LAS bf16x8*)a; }
DI bf16x4 ldstr(unsigned a) { return __builtin_amdgcn_ds_read_tr16_b64_v4bf16((LAS bf16x4*)a); }
DI bf16x8 cat(bf16x4 a, bf16x4 b) { return __builtin_shufflevector(a, b, 0, 1, 2, 3, 4, 5, 6, 7); }
DI void barrier() { asm volatile("s_barrier" ::: "memory"); }
template <int N> DI void wait_vm() { asm volatile("s_waitcnt vmcnt(%0)" :: "i"(N) : "memory"); }
template <bool NTP> DI void dma(unsigned lds, unsigned voff, __amdgpu_buffer_rsrc_t rs, unsigned soff) {
  if constexpr (NTP) asm volatile("s_mov_b32 m0, %0\n\ts_nop 0\n\tbuffer_load_dwordx4 %1, %2, %3 offen nt lds" :: "s"(lds), "v"(voff), "s"(rs), "s"(soff) : "memory");
  else               asm volatile("s_mov_b32 m0, %0\n\ts_nop 0\n\tbuffer_load_dwordx4 %1, %2, %3 offen lds" :: "s"(lds), "v"(voff), "s"(rs), "s"(soff) : "memory");
}
DI unsigned lane_id() { unsigned l; asm volatile("v_mbcnt_lo_u32_b32 %0, -1, 0\n\tv_mbcnt_hi_u32_b32 %0, -1, %0" : "=v"(l)); return l; }
DI unsigned swk(unsigned r4) { return (4u - r4) & 3u; }

constexpr int hu_cum(int i) { return i < 7 ? 0 : (i - 6 > 11 ? 11 : i - 6); }
constexpr int hu_at(int i) { return i < 0 ? 0 : hu_cum(i) - (i > 0 ? hu_cum(i - 1) : 0); }
constexpr unsigned blk_addr(int idx) {
  return idx == 0 ? SPARE : idx == 1 ? SPARE + 8192
       : idx == 2 ? KRING + (26 % 6) * 8192 : idx == 3 ? KRING + (27 % 6) * 8192
       : (idx & 1) == 0 ? KRING + ((28 + (idx - 4) / 2) % 6) * 8192 : VRING + ((26 + (idx - 5) / 2) % 6) * 8192;
}
constexpr int n_pre(int t) { return (t >= 0 && t < 24 ? 2 : 0) + (t == 24 || t == 25 ? 2 : 0); }
constexpr int n_v(int t) { return (t >= 0 && t + 5 <= 31) ? 2 : 0; }
constexpr int n_post(int t) { return (t + 7 <= 31 ? 2 : 0) + (t >= 26 ? 4 : 0); }
constexpr int n_all(int t) { return n_pre(t) + n_v(t) + n_post(t); }
constexpr int wait_b(int t) {
  int n = 0;
  if (t + 1 <= 4) { n = 2 * (10 - (2 * (t + 1) + 2)); for (int u = -1; u < t; ++u) n += n_all(u); }
  else { n = n_post(t - 4); for (int u = t - 3; u < t; ++u) n += n_all(u); }
  return n;
}

#define STAMP(i) do { } while (0)

template <int I> struct IC { static constexpr int v = I; };
template <int N, int I = 0, class F> DI void sfor(F&& f) { if constexpr (I < N) { f(IC<I>{}); sfor<N, I + 1>(f); } }

template <int NT> DI void compute_wave(const unsigned w, const unsigned lane, char* part, unsigned long long* stamps) {
  const unsigned r = lane & 15, g = lane >> 4, q4 = (lane >> 2) & 3, p4 = lane & 3;
  {
    f32x4 acc[4][5];
#pragma unroll
    for (int t = 0; t < 4; ++t)
#pragma unroll
      for (int pt = 0; pt < 5; ++pt) acc[t][pt] = (f32x4){0.f, 0.f, 0.f, 0.f};
    const unsigned wa0 = WRING + w * 4096 + g * 1024 + q4 * 64 + 32 * (g & 1) + 8 * p4;
    const unsigned wa1 = WRING + w * 4096 + g * 1024 + q4 * 64 + 32 * ((g & 1) ^ 1) + 8 * p4;
    const unsigned xa = WRING + XOFF + r * 64 + 16 * (g ^ swk(r >> 2));
#pragma unroll 1
    for (int i3 = 0; i3 < 6; ++i3) {
#pragma unroll
      for (int s = 0; s < 3; ++s) {
        barrier();
        bf16x8 a[4], x[5];
#pragma unroll
        for (int t = 0; t < 4; ++t) { const unsigned b = ((t & 1) ? wa1 : wa0) + s * WSLOT + (t >> 1) * 512; a[t] = cat(ldstr(b), ldstr(b + 256)); }
#pragma unroll
        for (int pt = 0; pt < 5; ++pt) x[pt] = ldsr128(xa + s * WSLOT + pt * 1024);
#pragma unroll
        for (int pt = 0; pt < 5; ++pt)
#pragma unroll
          for (int t = 0; t < 4; ++t) acc[t][pt] = MFMA(a[t], x[pt], acc[t][pt]);
      }
    }
    STAMP(1);
    const float c = 0.12751743082459868f;
    const unsigned h = w >> 1, dh = w & 1;
#pragma unroll
    for (int pt = 0; pt < 5; ++pt)
#pragma unroll
      for (int t = 0; t < 4; ++t) {
        u32x2 v = {cvtpk(acc[t][pt][0] * c, acc[t][pt][1] * c), cvtpk(acc[t][pt][2] * c, acc[t][pt][3] * c)};
        const unsigned a = QAREA + ((h * 5 + pt) * 4 + 2 * dh + (t >> 1)) * 1024 + (16 * (2 * (t & 1) + (g >> 1)) + r) * 16 + 8 * (g & 1);
        *(LAS u32x2*)a = v;
      }
    asm volatile("s_waitcnt lgkmcnt(0)" ::: "memory");
  }
  barrier();
  const unsigned T0 = NT == 3 ? w * 5 : (w - 2) * 5 + 3;
  bf16x8 Q[NT][4];
#pragma unroll
  for (int T = 0; T < NT; ++T)
#pragma unroll
    for (int ks = 0; ks < 4; ++ks) Q[T][ks] = ldsr128(QAREA + ((T0 + T) * 4 + ks) * 1024 + lane * 16);
  const unsigned ka = KRING + (r >> 3) * 2048 + (r & 7) * 64 + 16 * (g ^ swk(r >> 2));
  const unsigned va0 = VRING + (g >> 1) * 2048 + (4 * (g & 1) + q4) * 64 + 32 * (g & 1) + 8 * p4;
  const unsigned va1 = VRING + (g >> 1) * 2048 + (4 * (g & 1) + q4) * 64 + 32 * ((g & 1) ^ 1) + 8 * p4;
  f32x4 O[8][NT], L[NT], NM[NT], S[2][NT];
  u32x4 P[NT];
  bf16x8 Kf[4][2];
  const __bf16 one = (__bf16)1.0f;
  bf16x8 ones = {one, one, one, one, one, one, one, one};
  asm volatile("" : "+v"(ones));
#pragma unroll
  for (int T = 0; T < NT; ++T) {
    L[T] = (f32x4){0.f, 0.f, 0.f, 0.f};
#pragma unroll
    for (int db = 0; db < 8; ++db) O[db][T] = (f32x4){0.f, 0.f, 0.f, 0.f};
  }
  {
#pragma unroll
    for (int ks = 0; ks < 4; ++ks)
#pragma unroll
      for (int kt = 0; kt < 2; ++kt) Kf[ks][kt] = ldsr128(ka + kt * 4096 + ks * 512);
#pragma unroll
    for (int T = 0; T < NT; ++T)
#pragma unroll
      for (int kt = 0; kt < 2; ++kt) {
        f32x4 s = {0.f, 0.f, 0.f, 0.f};
#pragma unroll
        for (int ks = 0; ks < 4; ++ks) s = MFMA(Kf[ks][kt], Q[T][ks], s);
        S[kt][T] = s;
      }
    barrier();
#pragma unroll
    for (int ks = 0; ks < 2; ++ks)
#pragma unroll
      for (int kt = 0; kt < 2; ++kt) Kf[ks][kt] = ldsr128(ka + 8192 + kt * 4096 + ks * 512);
#pragma unroll
    for (int T = 0; T < NT; ++T) {
      float m = fmaxf(fmaxf(fmaxf(S[0][T][0], S[0][T][1]), fmaxf(S[0][T][2], S[0][T][3])), fmaxf(fmaxf(S[1][T][0], S[1][T][1]), fmaxf(S[1][T][2], S[1][T][3])));
      m = fmaxf(m, __shfl_xor(m, 16)); m = fmaxf(m, __shfl_xor(m, 32));
      NM[T] = (f32x4){-m, -m, -m, -m};
      asm volatile("" : "+v"(NM[T]));
#pragma unroll
      for (int kt = 0; kt < 2; ++kt)
#pragma unroll
        for (int i = 0; i < 4; ++i) S[kt][T][i] = __builtin_amdgcn_exp2f(S[kt][T][i] - m);
      P[T] = (u32x4){cvtpk(S[0][T][0], S[0][T][1]), cvtpk(S[0][T][2], S[0][T][3]), cvtpk(S[1][T][0], S[1][T][1]), cvtpk(S[1][T][2], S[1][T][3])};
    }
  }
  STAMP(2);
  unsigned ko = 8192, vo = 0;
  GAP();
#pragma unroll 1
  for (int t = 0; t < 31; ++t) {
    const unsigned kb = ka + ko, vb0 = va0 + vo, vb1 = va1 + vo;
    ko = ko == 5 * 8192 ? 0 : ko + 8192; vo = vo == 5 * 8192 ? 0 : vo + 8192;
    const unsigned kn = ka + ko;
    bf16x4 Vl[8], Vh[8];
    constexpr int NQ = 8 * NT, NE = 8 * NT;
    sfor<NQ>([&](auto mc) {
      constexpr int m = decltype(mc)::v, ks = m / (2 * NT), T = (m % (2 * NT)) / 2, kt = m & 1;
      S[kt][T] = MFMA(Kf[ks][kt], Q[T][ks], ks == 0 ? NM[T] : S[kt][T]);
      if constexpr (m < 4) Kf[2 + (m >> 1)][m & 1] = ldsr128(kb + (m & 1) * 4096 + (2 + (m >> 1)) * 512);
      constexpr int lo = m < 4 ? 0 : (16 * (m - 4)) / (NQ - 4), hi = m < 4 ? 0 : (16 * (m - 3)) / (NQ - 4);
      sfor<hi - lo>([&](auto ec) { constexpr int e = lo + decltype(ec)::v, db = e >> 1;
        if constexpr (e & 1) Vh[db] = ldstr(((db & 1) ? vb1 : vb0) + 4096 + (db >> 1) * 512); else Vl[db] = ldstr(((db & 1) ? vb1 : vb0) + (db >> 1) * 512); });
      GAP();
    });
    constexpr int NP = 9 * NT;
    sfor<NP>([&](auto mc) {
      constexpr int m = decltype(mc)::v, T = m % NT, db = m / NT - 1;
      if constexpr (db < 0) L[T] = MFMA(ones, __builtin_bit_cast(bf16x8, P[T]), L[T]);
      else O[db][T] = MFMA(cat(Vl[db], Vh[db]), __builtin_bit_cast(bf16x8, P[T]), O[db][T]);
      if constexpr (m < NE) { constexpr int eT = m / 8, ekt = (m >> 2) & 1, ei = m & 3; S[ekt][eT][ei] = __builtin_amdgcn_exp2f(S[ekt][eT][ei]); }
      if constexpr (m == 5 * NT) barrier();
      if constexpr (m > 5 * NT && m <= 5 * NT + 4) { constexpr int e = m - 5 * NT - 1; Kf[e >> 1][e & 1] = ldsr128(kn + (e & 1) * 4096 + (e >> 1) * 512); }
      if constexpr (db == 7) P[T] = (u32x4){cvtpk(S[0][T][0], S[0][T][1]), cvtpk(S[0][T][2], S[0][T][3]), cvtpk(S[1][T][0], S[1][T][1]), cvtpk(S[1][T][2], S[1][T][3])};
      GAP();
    });
  }
  const unsigned lane2 = lane_id();
  { const unsigned lane = lane2; STAMP(3); }
  {
    const unsigned lane = lane2;
    const unsigned vb0 = va0 + vo, vb1 = va1 + vo;
    bf16x8 Vf[8];
#pragma unroll
    for (int db = 0; db < 8; ++db) { const unsigned b = ((db & 1) ? vb1 : vb0) + (db >> 1) * 512; Vf[db] = cat(ldstr(b), ldstr(b + 4096)); }
#pragma unroll
    for (int T = 0; T < NT; ++T) L[T] = MFMA(ones, __builtin_bit_cast(bf16x8, P[T]), L[T]);
#pragma unroll
    for (int db = 0; db < 8; ++db)
#pragma unroll
      for (int T = 0; T < NT; ++T) O[db][T] = MFMA(Vf[db], __builtin_bit_cast(bf16x8, P[T]), O[db][T]);
#pragma unroll
    for (int T = 0; T < NT; ++T) {
      const float il = 1.0f / L[T][0];
#pragma unroll
      for (int s = 0; s < 4; ++s) {
        const f32x4 a = O[2 * s][T], b = O[2 * s + 1][T];
        u32x4 v = {cvtpk(a[0] * il, a[1] * il), cvtpk(a[2] * il, a[3] * il), cvtpk(b[0] * il, b[1] * il), cvtpk(b[2] * il, b[3] * il)};
        *(LAS u32x4*)(QAREA + ((T0 + T) * 4 + s) * 1024 + lane * 16) = v;
      }
    }
    asm volatile("s_waitcnt lgkmcnt(0)" ::: "memory");
  }
  barrier();
  {
    const unsigned lane = lane2, g = lane >> 4, q4 = (lane >> 2) & 3, p4 = lane & 3;
    STAMP(4);
    unsigned wb[3][2];
#pragma unroll
    for (int i = 0; i < 3; ++i) {
      const unsigned tau = 3 * w + i, pp = tau >> 1, u = tau & 1;
      unsigned b0 = blk_addr(0), b1 = blk_addr(6);
#pragma unroll
      for (int k = 1; k < 6; ++k) { b0 = pp == (unsigned)k ? blk_addr(k) : b0; b1 = pp == (unsigned)k ? blk_addr(6 + k) : b1; }
      const unsigned la = (4 * g + q4) * 64 + 32 * (u ^ (g & 1)) + 8 * p4;
      wb[i][0] = b0 + la; wb[i][1] = b1 + la;
    }
    f32x4 acc[3][5];
#pragma unroll
    for (int i = 0; i < 3; ++i)
#pragma unroll
      for (int pt = 0; pt < 5; ++pt) acc[i][pt] = (f32x4){0.f, 0.f, 0.f, 0.f};
#pragma unroll
    for (int s = 0; s < 8; ++s) {
      bf16x8 a[3], o[5];
#pragma unroll
      for (int i = 0; i < 3; ++i) { const unsigned b = wb[i][s >> 2] + (s & 3) * 2048; a[i] = cat(ldstr(b), ldstr(b + 1024)); }
#pragma unroll
      for (int pt = 0; pt < 5; ++pt) o[pt] = ldsr128(QAREA + (((s >> 2) * 5 + pt) * 4 + (s & 3)) * 1024 + lane * 16);
#pragma unroll
      for (int pt = 0; pt < 5; ++pt)
#pragma unroll
        for (int i = 0; i < 3; ++i) acc[i][pt] = MFMA(a[i], o[pt], acc[i][pt]);
    }
    const __amdgpu_buffer_rsrc_t rp = __builtin_amdgcn_make_buffer_rsrc(part, 0, PART_BYTES, 0x00020000);
    const unsigned fb = (60 + w * 7) * 1024;
#pragma unroll
    for (int pt = 0; pt < 5; ++pt) {
      u32x4 v = {cvtpk(acc[0][pt][0], acc[0][pt][1]), cvtpk(acc[0][pt][2], acc[0][pt][3]), cvtpk(acc[1][pt][0], acc[1][pt][1]), cvtpk(acc[1][pt][2], acc[1][pt][3])};
      __builtin_amdgcn_raw_buffer_store_b128(v, rp, lane * 16, fb + pt * 1024, 16);
    }
#pragma unroll
    for (int f = 0; f < 2; ++f) {
      u32x4 v = {cvtpk(acc[2][2 * f][0], acc[2][2 * f][1]), cvtpk(acc[2][2 * f][2], acc[2][2 * f][3]), cvtpk(acc[2][2 * f + 1][0], acc[2][2 * f + 1][1]), cvtpk(acc[2][2 * f + 1][2], acc[2][2 * f + 1][3])};
      __builtin_amdgcn_raw_buffer_store_b128(v, rp, lane * 16, fb + (5 + f) * 1024, 16);
    }
    u32x2 v2 = {cvtpk(acc[2][4][0], acc[2][4][1]), cvtpk(acc[2][4][2], acc[2][4][3])};
    __builtin_amdgcn_raw_buffer_store_b64(v2, rp, lane * 8, 88 * 1024 + w * 512, 16);
  }
}

DI void helper_wave(const unsigned j, const unsigned lane, const unsigned b, const unsigned kvh, const char* x, const char* kc, const char* vc, const char* wq, const char* wo, char* part, unsigned long long* stamps) {
  const __amdgpu_buffer_rsrc_t rx = __builtin_amdgcn_make_buffer_rsrc((void*)(x + (size_t)b * 92160), 0, 92160, 0x00020000);
  const __amdgpu_buffer_rsrc_t rk = __builtin_amdgcn_make_buffer_rsrc((void*)(kc + (size_t)(b * 16 + kvh) * 262144), 0, 262144, 0x00020000);
  const __amdgpu_buffer_rsrc_t rv = __builtin_amdgcn_make_buffer_rsrc((void*)(vc + (size_t)(b * 16 + kvh) * 262144), 0, 262144, 0x00020000);
  const __amdgpu_buffer_rsrc_t rq = __builtin_amdgcn_make_buffer_rsrc((void*)(wq + (size_t)kvh * 512), 0, 576 * 8192 - kvh * 512, 0x00020000);
  const __amdgpu_buffer_rsrc_t ro = __builtin_amdgcn_make_buffer_rsrc((void*)(wo + (size_t)kvh * 256 * 1152), 0, 256 * 1152, 0x00020000);
  const unsigned cb = lane >> 5, row = (lane >> 2) & 7, c4 = lane & 3, r16 = lane >> 2;
  const unsigned vk = (8 * j + row) * 256 + cb * 64 + 16 * (c4 ^ swk(2 * (j & 1) + (row >> 2)));
  const unsigned vv = (8 * j + row) * 256 + cb * 64 + 16 * (c4 ^ (2 * (row >> 2)));
  const unsigned vq = (8 * j + row) * 8192 + cb * 64 + 16 * (c4 ^ (2 * (j & 1)));
  const unsigned vx = (16 * j + r16) * 1152 + 16 * (c4 ^ swk(r16 >> 2));
  const unsigned vx4 = (64 + 4 * j + (r16 & 3)) * 1152 + 16 * (c4 ^ swk(j));
  const unsigned vo = r16 * 1152 + 16 * (c4 ^ (2 * ((r16 >> 2) & 1)));
  auto dma_k = [&](int t) { dma<KV_NT>(KRING + (t % 6) * 8192 + j * 2048, vk, rk, t * 8192); dma<KV_NT>(KRING + (t % 6) * 8192 + j * 2048 + 1024, vk, rk, t * 8192 + 128); };
  auto dma_v = [&](int t) { dma<KV_NT>(VRING + (t % 6) * 8192 + j * 2048, vv, rv, t * 8192); dma<KV_NT>(VRING + (t % 6) * 8192 + j * 2048 + 1024, vv, rv, t * 8192 + 128); };
  unsigned ksn = (b * 9 / 8 + 9 * (kvh & 1)) % 18;
  auto dma_w = [&](int i) {
    const unsigned slot = WRING + (i % 3) * WSLOT;
    dma<false>(slot + XOFF + j * 1024, vx, rx, ksn * 64);
    if (lane < 16) dma<false>(slot + XOFF + 4096 + j * 256, vx4, rx, ksn * 64);
#pragma unroll
    for (int ns = 0; ns < 4; ++ns) dma<false>(slot + (ns * 4) * 1024 + j * 1024, vq, rq, ksn * 262144 + ns * 128);
    ksn = ksn == 17 ? 0 : ksn + 1;
  };
  auto dma_hu = [&](int pos) { if (pos == 0) dma_k(0); else if (pos & 1) dma_k((pos + 1) / 2); else dma_v(pos / 2 - 1); };
  asm volatile("s_nop 4" ::: "memory");
  dma_w(0); dma_w(1); dma_w(2);
  sfor<18>([&](auto ic) {
    constexpr int i = decltype(ic)::v;
    constexpr int n = i == 0 ? 12 : i == 1 ? 6 + 2 * hu_at(0) : i == 2 ? 2 * hu_at(0) + 6 + 2 * hu_at(1) : 2 * hu_at(i - 2) + (i + 1 < 18 ? 6 : 0) + 2 * hu_at(i - 1);
    wait_vm<n>();
    barrier();
    if constexpr (i >= 1 && i + 2 < 18) dma_w(i + 2);
    sfor<hu_at(i)>([&](auto ec) { dma_hu(hu_cum(i) - hu_at(i) + decltype(ec)::v); });
  });
  wait_vm<20>();
  barrier();
  bf16x8 W[3][2][8];
  const unsigned g = lane >> 4, q4 = (lane >> 2) & 3, p4 = lane & 3;
  const unsigned sa0 = QAREA + j * 10240 + (4 * g + q4) * 64 + 32 * (g & 1) + 8 * p4, sa1 = QAREA + j * 10240 + (4 * g + q4) * 64 + 32 * ((g & 1) ^ 1) + 8 * p4;
  auto dma_blk = [&](int idx) {
    const unsigned so = ((idx / 6) * 128 + 32 * j) * 1152 + (384 + 32 * (idx % 6)) * 2;
    dma<false>(blk_addr(idx) + j * 2048, vo, ro, so); dma<false>(blk_addr(idx) + j * 2048 + 1024, vo, ro, so + 16 * 1152);
  };
  sfor<32>([&](auto tc) {
    constexpr int t = decltype(tc)::v - 1;
    wait_vm<wait_b(t)>();
    barrier();
    if constexpr (t >= 0 && t < 24) {
      asm volatile("s_waitcnt lgkmcnt(0)" ::: "memory");
      const unsigned so = (32 * (t % 8)) * 1152 + (96 * j + 32 * (t / 8)) * 2;
      dma<false>(QAREA + j * 10240 + (t % 5) * 2048, vo, ro, so); dma<false>(QAREA + j * 10240 + (t % 5) * 2048 + 1024, vo, ro, so + 16 * 1152);
    }
    if constexpr (t == 24 || t == 25) dma_blk(t - 24);
    if constexpr (n_v(t) > 0) dma_v(t + 5);
    if constexpr (t + 7 <= 31) dma_k(t + 7);
    if constexpr (t >= 26) { dma_blk(2 * (t - 25)); dma_blk(2 * (t - 25) + 1); }
    if constexpr (t >= 4 && t < 28) {
      constexpr int u = t - 4;
      W[u / 8][0][u % 8] = cat(ldstr(sa0 + (u % 5) * 2048), ldstr(sa0 + (u % 5) * 2048 + 1024));
      W[u / 8][1][u % 8] = cat(ldstr(sa1 + (u % 5) * 2048), ldstr(sa1 + (u % 5) * 2048 + 1024));
      asm volatile("" : "+v"(W[u / 8][0][u % 8]), "+v"(W[u / 8][1][u % 8]));
    }
  });
  wait_vm<0>();
  barrier();
  const __amdgpu_buffer_rsrc_t rp = __builtin_amdgcn_make_buffer_rsrc(part, 0, PART_BYTES, 0x00020000);
#pragma unroll
  for (int p = 0; p < 3; ++p) {
    f32x4 acc[2][5];
#pragma unroll
    for (int pt = 0; pt < 5; ++pt) { acc[0][pt] = (f32x4){0.f, 0.f, 0.f, 0.f}; acc[1][pt] = (f32x4){0.f, 0.f, 0.f, 0.f}; }
#pragma unroll
    for (int s = 0; s < 8; ++s)
#pragma unroll
      for (int pt = 0; pt < 5; ++pt) {
        const bf16x8 o = ldsr128(QAREA + (((s >> 2) * 5 + pt) * 4 + (s & 3)) * 1024 + lane * 16);
        acc[0][pt] = MFMA(W[p][0][s], o, acc[0][pt]); acc[1][pt] = MFMA(W[p][1][s], o, acc[1][pt]);
      }
#pragma unroll
    for (int pt = 0; pt < 5; ++pt) {
      u32x4 v = {cvtpk(acc[0][pt][0], acc[0][pt][1]), cvtpk(acc[0][pt][2], acc[0][pt][3]), cvtpk(acc[1][pt][0], acc[1][pt][1]), cvtpk(acc[1][pt][2], acc[1][pt][3])};
      __builtin_amdgcn_raw_buffer_store_b128(v, rp, lane * 16, ((j * 3 + p) * 5 + pt) * 1024, 16);
    }
    asm volatile("" ::: "memory"); GAP();
  }
}

__global__ __launch_bounds__(512, 2) void gqa_main(const char* __restrict__ x, const char* __restrict__ kc, const char* __restrict__ vc, const char* __restrict__ wq, const char* __restrict__ wo, char* __restrict__ ws) {
  extern __shared__ __attribute__((aligned(16))) char smem[];
  const unsigned lane = threadIdx.x & 63, w = rfl(threadIdx.x >> 6);
  const unsigned wg = blockIdx.x, kvh = 2 * (wg & 7) + ((wg >> 3) & 1), b = wg >> 4;
  char* part = ws + (size_t)(b * 16 + kvh) * PART_BYTES;
  unsigned long long* stamps = nullptr; unsigned long long* cst = nullptr;
  if (w >= 4) helper_wave(w - 4, lane, b, kvh, x, kc, vc, wq, wo, part, stamps);
  else if (w < 2) compute_wave<3>(w, lane, part, cst);
  else compute_wave<2>(w, lane, part, cst);
}

__global__ __launch_bounds__(384) void gqa_reduce(const char* __restrict__ ws, unsigned short* __restrict__ out) {
  const unsigned wg = blockIdx.x, tid = threadIdx.x, b = wg >> 4, c = wg & 15;
  if (tid >= 360) return;
  const unsigned G = 360 * c + tid;
  const u32x4* src = (const u32x4*)(ws + (size_t)b * 16 * PART_BYTES) + G;
  u32x4 v[16];
#pragma unroll
  for (int k = 0; k < 16; ++k) v[k] = src[k * (PART_BYTES / 16)];
  float a[8] = {0.f, 0.f, 0.f, 0.f, 0.f, 0.f, 0.f, 0.f};
#pragma unroll
  for (int k = 0; k < 16; ++k)
#pragma unroll
    for (int e = 0; e < 4; ++e) { a[2 * e] += __uint_as_float(v[k][e] << 16); a[2 * e + 1] += __uint_as_float(v[k][e] & 0xffff0000u); }
  const unsigned F = G >> 6, l = G & 63, g = l >> 4, r = l & 15;
  unsigned n0, n1, pos0, pos1;
  if (F < 60) { n0 = 32 * (F / 5) + 4 * g; n1 = n0 + 16; pos0 = pos1 = 16 * (F % 5) + r; }
  else if (F < 88) { const unsigned w = (F - 60) / 7, f = (F - 60) % 7;
    if (f < 5) { n0 = 384 + 48 * w + 4 * g; n1 = n0 + 16; pos0 = pos1 = 16 * f + r; }
    else { n0 = n1 = 384 + 48 * w + 32 + 4 * g; pos0 = 32 * (f - 5) + r; pos1 = pos0 + 16; } }
  else { const unsigned G2 = G - 88 * 64, w = G2 >> 5, l0 = 2 * (G2 & 31); n0 = n1 = 384 + 48 * w + 32 + 4 * (l0 >> 4); pos0 = 64 + (l0 & 15); pos1 = pos0 + 1; }
  *(u32x2*)(out + (size_t)(b * 80 + pos0) * 576 + n0) = (u32x2){cvtpk(a[0], a[1]), cvtpk(a[2], a[3])};
  *(u32x2*)(out + (size_t)(b * 80 + pos1) * 576 + n1) = (u32x2){cvtpk(a[4], a[5]), cvtpk(a[6], a[7])};
}

typedef unsigned long long pu64;
__global__ __launch_bounds__(256) void probe_stream(const u32x4* __restrict__ k, const u32x4* __restrict__ v, pu64* ts) {
  extern __shared__ __attribute__((aligned(16))) char smem[];
  const int wg0 = blockIdx.x; const int tid = threadIdx.x; const int lane = tid & 63;
  const int wg = (wg0 >> 4) * 16 + 2 * (wg0 & 7) + ((wg0 >> 3) & 1);
  const int w = __builtin_amdgcn_readfirstlane(tid >> 6);
  pu64 t0 = __builtin_amdgcn_s_memrealtime();
  const u32x4* kp = k + (size_t)wg * 16384; const u32x4* vp = v + (size_t)wg * 16384;
  #pragma unroll 1
  for (int p = 0; p < 128; ++p) {
    const int tile = p >> 3, q = p & 7;
    if (1 == 1 && q >= 4) continue;
    if (1 == 2 && q < 4) continue;
    const u32x4* src = (q < 4 ? kp : vp) + tile * 1024 + (w * 4 + (q & 3)) * 64 + lane;
    char* dst = smem + ((p % 16) * 4 + w) * 1024;
    __builtin_amdgcn_global_load_lds((const __attribute__((address_space(1))) void*)src, (__attribute__((address_space(3))) void*)dst, 16, 0, 0);
    asm volatile("s_waitcnt vmcnt(15)" ::: "memory");
  }
  asm volatile("s_waitcnt vmcnt(0)" ::: "memory");
  __syncthreads();
  if (tid == 0) { ts[2 * wg] = t0; ts[2 * wg + 1] = __builtin_amdgcn_s_memrealtime(); }
}
__global__ void probe_spin(const pu64* ts) {
  pu64 mn = ~0ull, mx = 0, s01 = 0, s1415 = 0, sall = 0;
  for (int i = 0; i < 256; ++i) { mn = ts[2 * i] < mn ? ts[2 * i] : mn; mx = ts[2 * i + 1] > mx ? ts[2 * i + 1] : mx; }
  for (int i = 0; i < 256; ++i) { pu64 d = ts[2 * i + 1] - mn; if (i < 32) s01 += d; if (i >= 224) s1415 += d; sall += d; }
  pu64 M = mx - mn;
  if (M > 20000) M = 20000;
  pu64 t0 = __builtin_amdgcn_s_memrealtime();
  while (__builtin_amdgcn_s_memrealtime() - t0 < 50 * M) __builtin_amdgcn_s_sleep(8);
}

extern "C" void kernel_launch(void* const* d_in, const int* in_sizes, int n_in, void* d_out, int out_size, void* d_ws, size_t ws_size, hipStream_t stream) {
  (void)hipFuncSetAttribute((const void*)gqa_main, hipFuncAttributeMaxDynamicSharedMemorySize, (int)LDS_BYTES);
  probe_stream<<<256, 256, 65536, stream>>>((const u32x4*)d_in[1], (const u32x4*)d_in[2], (pu64*)((char*)d_ws + (100u << 20)));
  gqa_main<<<256, 512, LDS_BYTES, stream>>>((const char*)d_in[0], (const char*)d_in[1], (const char*)d_in[2], (const char*)d_in[3], (const char*)d_in[4], (char*)d_ws);
  gqa_reduce<<<256, 384, 0, stream>>>((const char*)d_ws, (unsigned short*)d_out);
  probe_spin<<<1, 1, 0, stream>>>((const pu64*)((char*)d_ws + (100u << 20)));
}
```

```cpp
#define STAMPS
#include <hip/hip_runtime.h>
#include <hip/hip_bf16.h>

typedef __bf16 bf16x8 __attribute__((ext_vector_type(8)));
typedef __bf16 bf16x4 __attribute__((ext_vector_type(4)));
typedef __bf16 bf16x2 __attribute__((ext_vector_type(2)));
typedef float f32x4 __attribute__((ext_vector_type(4)));
typedef float f32x2 __attribute__((ext_vector_type(2)));
typedef unsigned u32x4 __attribute__((ext_vector_type(4)));
typedef unsigned u32x2 __attribute__((ext_vector_type(2)));
#define LAS __attribute__((address_space(3)))
#define DI __device__ __forceinline__
#define GAP() __builtin_amdgcn_sched_barrier(0)
#define MFMA(a, b, c) __builtin_amdgcn_mfma_f32_16x16x32_bf16((a), (b), (c), 0, 0, 0)

#define KV_NT 1
#define NSTAMP 8
#define STAMP_OFF (64u << 20)

constexpr unsigned KRING = 0, VRING = 49152, WRING = 98304, WSLOT = 21504, XOFF = 16384;
constexpr unsigned QAREA = WRING;
constexpr unsigned SPARE = WRING + 40960;
constexpr unsigned LDS_BYTES = 163840;
constexpr int PART_BYTES = 92160;

DI unsigned rfl(unsigned x) { return __builtin_amdgcn_readfirstlane(x); }
DI unsigned cvtpk(float lo, float hi) { f32x2 v = {lo, hi}; bf16x2 b = __builtin_convertvector(v, bf16x2); return __builtin_bit_cast(unsigned, b); }
DI bf16x8 ldsr128(unsigned a) { return *(const LAS bf16x8*)a; }
DI bf16x4 ldstr(unsigned a) { return __builtin_amdgcn_ds_read_tr16_b64_v4bf16((LAS bf16x4*)a); }
DI bf16x8 cat(bf16x4 a, bf16x4 b) { return __builtin_shufflevector(a, b, 0, 1, 2, 3, 4, 5, 6, 7); }
DI void barrier() { asm volatile("s_barrier" ::: "memory"); }
template <int N> DI void wait_vm() { asm volatile("s_waitcnt vmcnt(%0)" :: "i"(N) : "memory"); }
template <bool NTP> DI void dma(unsigned lds, unsigned voff, __amdgpu_buffer_rsrc_t rs, unsigned soff) {
  if constexpr (NTP) asm volatile("s_mov_b32 m0, %0\n\ts_nop 0\n\tbuffer_load_dwordx4 %1, %2, %3 offen nt lds" :: "s"(lds), "v"(voff), "s"(rs), "s"(soff) : "memory");
  else               asm volatile("s_mov_b32 m0, %0\n\ts_nop 0\n\tbuffer_load_dwordx4 %1, %2, %3 offen lds" :: "s"(lds), "v"(voff), "s"(rs), "s"(soff) : "memory");
}
DI unsigned lane_id() { unsigned l; asm volatile("v_mbcnt_lo_u32_b32 %0, -1, 0\n\tv_mbcnt_hi_u32_b32 %0, -1, %0" : "=v"(l)); return l; }
DI unsigned swk(unsigned r4) { return (4u - r4) & 3u; }

constexpr int hu_cum(int i) { return i < 7 ? 0 : (i - 6 > 11 ? 11 : i - 6); }
constexpr int hu_at(int i) { return i < 0 ? 0 : hu_cum(i) - (i > 0 ? hu_cum(i - 1) : 0); }
constexpr unsigned blk_addr(int idx) {
  return idx == 0 ? SPARE : idx == 1 ? SPARE + 8192
       : idx == 2 ? KRING + (26 % 6) * 8192 : idx == 3 ? KRING + (27 % 6) * 8192
       : (idx & 1) == 0 ? KRING + ((28 + (idx - 4) / 2) % 6) * 8192 : VRING + ((26 + (idx - 5) / 2) % 6) * 8192;
}
constexpr int n_pre(int t) { return (t >= 0 && t < 24 ? 2 : 0) + (t == 24 || t == 25 ? 2 : 0); }
constexpr int n_v(int t) { return (t >= 0 && t + 5 <= 31) ? 2 : 0; }
constexpr int n_post(int t) { return (t + 7 <= 31 ? 2 : 0) + (t >= 26 ? 4 : 0); }
constexpr int n_all(int t) { return n_pre(t) + n_v(t) + n_post(t); }
constexpr int wait_b(int t) {
  int n = 0;
  if (t + 1 <= 4) { n = 2 * (10 - (2 * (t + 1) + 2)); for (int u = -1; u < t; ++u) n += n_all(u); }
  else { n = n_post(t - 4); for (int u = t - 3; u < t; ++u) n += n_all(u); }
  return n;
}

#define STAMP(i) do { if (lane == 0) stamps[blockIdx.x * NSTAMP + (i)] = __builtin_amdgcn_s_memrealtime(); } while (0)

template <int I> struct IC { static constexpr int v = I; };
template <int N, int I = 0, class F> DI void sfor(F&& f) { if constexpr (I < N) { f(IC<I>{}); sfor<N, I + 1>(f); } }

template <int NT> DI void compute_wave(const unsigned w, const unsigned lane, char* part, unsigned long long* stamps) {
  const unsigned r = lane & 15, g = lane >> 4, q4 = (lane >> 2) & 3, p4 = lane & 3;
  {
    f32x4 acc[4][5];
#pragma unroll
    for (int t = 0; t < 4; ++t)
#pragma unroll
      for (int pt = 0; pt < 5; ++pt) acc[t][pt] = (f32x4){0.f, 0.f, 0.f, 0.f};
    const unsigned wa0 = WRING + w * 4096 + g * 1024 + q4 * 64 + 32 * (g & 1) + 8 * p4;
    const unsigned wa1 = WRING + w * 4096 + g * 1024 + q4 * 64 + 32 * ((g & 1) ^ 1) + 8 * p4;
    const unsigned xa = WRING + XOFF + r * 64 + 16 * (g ^ swk(r >> 2));
#pragma unroll 1
    for (int i3 = 0; i3 < 6; ++i3) {
#pragma unroll
      for (int s = 0; s < 3; ++s) {
        barrier();
        bf16x8 a[4], x[5];
#pragma unroll
        for (int t = 0; t < 4; ++t) { const unsigned b = ((t & 1) ? wa1 : wa0) + s * WSLOT + (t >> 1) * 512; a[t] = cat(ldstr(b), ldstr(b + 256)); }
#pragma unroll
        for (int pt = 0; pt < 5; ++pt) x[pt] = ldsr128(xa + s * WSLOT + pt * 1024);
#pragma unroll
        for (int pt = 0; pt < 5; ++pt)
#pragma unroll
          for (int t = 0; t < 4; ++t) acc[t][pt] = MFMA(a[t], x[pt], acc[t][pt]);
      }
    }
    STAMP(1);
    const float c = 0.12751743082459868f;
    const unsigned h = w >> 1, dh = w & 1;
#pragma unroll
    for (int pt = 0; pt < 5; ++pt)
#pragma unroll
      for (int t = 0; t < 4; ++t) {
        u32x2 v = {cvtpk(acc[t][pt][0] * c, acc[t][pt][1] * c), cvtpk(acc[t][pt][2] * c, acc[t][pt][3] * c)};
        const unsigned a = QAREA + ((h * 5 + pt) * 4 + 2 * dh + (t >> 1)) * 1024 + (16 * (2 * (t & 1) + (g >> 1)) + r) * 16 + 8 * (g & 1);
        *(LAS u32x2*)a = v;
      }
    asm volatile("s_waitcnt lgkmcnt(0)" ::: "memory");
  }
  barrier();
  const unsigned T0 = NT == 3 ? w * 5 : (w - 2) * 5 + 3;
  bf16x8 Q[NT][4];
#pragma unroll
  for (int T = 0; T < NT; ++T)
#pragma unroll
    for (int ks = 0; ks < 4; ++ks) Q[T][ks] = ldsr128(QAREA + ((T0 + T) * 4 + ks) * 1024 + lane * 16);
  const unsigned ka = KRING + (r >> 3) * 2048 + (r & 7) * 64 + 16 * (g ^ swk(r >> 2));
  const unsigned va0 = VRING + (g >> 1) * 2048 + (4 * (g & 1) + q4) * 64 + 32 * (g & 1) + 8 * p4;
  const unsigned va1 = VRING + (g >> 1) * 2048 + (4 * (g & 1) + q4) * 64 + 32 * ((g & 1) ^ 1) + 8 * p4;
  f32x4 O[8][NT], L[NT], NM[NT], S[2][NT];
  u32x4 P[NT];
  bf16x8 Kf[4][2];
  const __bf16 one = (__bf16)1.0f;
  bf16x8 ones = {one, one, one, one, one, one, one, one};
  asm volatile("" : "+v"(ones));
#pragma unroll
  for (int T = 0; T < NT; ++T) {
    L[T] = (f32x4){0.f, 0.f, 0.f, 0.f};
#pragma unroll
    for (int db = 0; db < 8; ++db) O[db][T] = (f32x4){0.f, 0.f, 0.f, 0.f};
  }
  {
#pragma unroll
    for (int ks = 0; ks < 4; ++ks)
#pragma unroll
      for (int kt = 0; kt < 2; ++kt) Kf[ks][kt] = ldsr128(ka + kt * 4096 + ks * 512);
#pragma unroll
    for (int T = 0; T < NT; ++T)
#pragma unroll
      for (int kt = 0; kt < 2; ++kt) {
        f32x4 s = {0.f, 0.f, 0.f, 0.f};
#pragma unroll
        for (int ks = 0; ks < 4; ++ks) s = MFMA(Kf[ks][kt], Q[T][ks], s);
        S[kt][T] = s;
      }
    barrier();
#pragma unroll
    for (int ks = 0; ks < 2; ++ks)
#pragma unroll
      for (int kt = 0; kt < 2; ++kt) Kf[ks][kt] = ldsr128(ka + 8192 + kt * 4096 + ks * 512);
#pragma unroll
    for (int T = 0; T < NT; ++T) {
      float m = fmaxf(fmaxf(fmaxf(S[0][T][0], S[0][T][1]), fmaxf(S[0][T][2], S[0][T][3])), fmaxf(fmaxf(S[1][T][0], S[1][T][1]), fmaxf(S[1][T][2], S[1][T][3])));
      m = fmaxf(m, __shfl_xor(m, 16)); m = fmaxf(m, __shfl_xor(m, 32));
      NM[T] = (f32x4){-m, -m, -m, -m};
      asm volatile("" : "+v"(NM[T]));
#pragma unroll
      for (int kt = 0; kt < 2; ++kt)
#pragma unroll
        for (int i = 0; i < 4; ++i) S[kt][T][i] = __builtin_amdgcn_exp2f(S[kt][T][i] - m);
      P[T] = (u32x4){cvtpk(S[0][T][0], S[0][T][1]), cvtpk(S[0][T][2], S[0][T][3]), cvtpk(S[1][T][0], S[1][T][1]), cvtpk(S[1][T][2], S[1][T][3])};
    }
  }
  STAMP(2);
  unsigned ko = 8192, vo = 0;
  GAP();
#pragma unroll 1
  for (int t = 0; t < 31; ++t) {
    const unsigned kb = ka + ko, vb0 = va0 + vo, vb1 = va1 + vo;
    ko = ko == 5 * 8192 ? 0 : ko + 8192; vo = vo == 5 * 8192 ? 0 : vo + 8192;
    const unsigned kn = ka + ko;
    bf16x4 Vl[8], Vh[8];
    constexpr int NQ = 8 * NT, NE = 8 * NT;
    sfor<NQ>([&](auto mc) {
      constexpr int m = decltype(mc)::v, ks = m / (2 * NT), T = (m % (2 * NT)) / 2, kt = m & 1;
      S[kt][T] = MFMA(Kf[ks][kt], Q[T][ks], ks == 0 ? NM[T] : S[kt][T]);
      if constexpr (m < 4) Kf[2 + (m >> 1)][m & 1] = ldsr128(kb + (m & 1) * 4096 + (2 + (m >> 1)) * 512);
      constexpr int lo = m < 4 ? 0 : (16 * (m - 4)) / (NQ - 4), hi = m < 4 ? 0 : (16 * (m - 3)) / (NQ - 4);
      sfor<hi - lo>([&](auto ec) { constexpr int e = lo + decltype(ec)::v, db = e >> 1;
        if constexpr (e & 1) Vh[db] = ldstr(((db & 1) ? vb1 : vb0) + 4096 + (db >> 1) * 512); else Vl[db] = ldstr(((db & 1) ? vb1 : vb0) + (db >> 1) * 512); });
      GAP();
    });
    constexpr int NP = 9 * NT;
    sfor<NP>([&](auto mc) {
      constexpr int m = decltype(mc)::v, T = m % NT, db = m / NT - 1;
      if constexpr (db < 0) L[T] = MFMA(ones, __builtin_bit_cast(bf16x8, P[T]), L[T]);
      else O[db][T] = MFMA(cat(Vl[db], Vh[db]), __builtin_bit_cast(bf16x8, P[T]), O[db][T]);
      if constexpr (m < NE) { constexpr int eT = m / 8, ekt = (m >> 2) & 1, ei = m & 3; S[ekt][eT][ei] = __builtin_amdgcn_exp2f(S[ekt][eT][ei]); }
      if constexpr (m == 5 * NT) barrier();
      if constexpr (m > 5 * NT && m <= 5 * NT + 4) { constexpr int e = m - 5 * NT - 1; Kf[e >> 1][e & 1] = ldsr128(kn + (e & 1) * 4096 + (e >> 1) * 512); }
      if constexpr (db == 7) P[T] = (u32x4){cvtpk(S[0][T][0], S[0][T][1]), cvtpk(S[0][T][2], S[0][T][3]), cvtpk(S[1][T][0], S[1][T][1]), cvtpk(S[1][T][2], S[1][T][3])};
      GAP();
    });
  }
  const unsigned lane2 = lane_id();
  { const unsigned lane = lane2; STAMP(3); }
  {
    const unsigned lane = lane2;
    const unsigned vb0 = va0 + vo, vb1 = va1 + vo;
    bf16x8 Vf[8];
#pragma unroll
    for (int db = 0; db < 8; ++db) { const unsigned b = ((db & 1) ? vb1 : vb0) + (db >> 1) * 512; Vf[db] = cat(ldstr(b), ldstr(b + 4096)); }
#pragma unroll
    for (int T = 0; T < NT; ++T) L[T] = MFMA(ones, __builtin_bit_cast(bf16x8, P[T]), L[T]);
#pragma unroll
    for (int db = 0; db < 8; ++db)
#pragma unroll
      for (int T = 0; T < NT; ++T) O[db][T] = MFMA(Vf[db], __builtin_bit_cast(bf16x8, P[T]), O[db][T]);
#pragma unroll
    for (int T = 0; T < NT; ++T) {
      const float il = 1.0f / L[T][0];
#pragma unroll
      for (int s = 0; s < 4; ++s) {
        const f32x4 a = O[2 * s][T], b = O[2 * s + 1][T];
        u32x4 v = {cvtpk(a[0] * il, a[1] * il), cvtpk(a[2] * il, a[3] * il), cvtpk(b[0] * il, b[1] * il), cvtpk(b[2] * il, b[3] * il)};
        *(LAS u32x4*)(QAREA + ((T0 + T) * 4 + s) * 1024 + lane * 16) = v;
      }
    }
    asm volatile("s_waitcnt lgkmcnt(0)" ::: "memory");
  }
  barrier();
  {
    const unsigned lane = lane2, g = lane >> 4, q4 = (lane >> 2) & 3, p4 = lane & 3;
    STAMP(4);
    unsigned wb[3][2];
#pragma unroll
    for (int i = 0; i < 3; ++i) {
      const unsigned tau = 3 * w + i, pp = tau >> 1, u = tau & 1;
      unsigned b0 = blk_addr(0), b1 = blk_addr(6);
#pragma unroll
      for (int k = 1; k < 6; ++k) { b0 = pp == (unsigned)k ? blk_addr(k) : b0; b1 = pp == (unsigned)k ? blk_addr(6 + k) : b1; }
      const unsigned la = (4 * g + q4) * 64 + 32 * (u ^ (g & 1)) + 8 * p4;
      wb[i][0] = b0 + la; wb[i][1] = b1 + la;
    }
    f32x4 acc[3][5];
#pragma unroll
    for (int i = 0; i < 3; ++i)
#pragma unroll
      for (int pt = 0; pt < 5; ++pt) acc[i][pt] = (f32x4){0.f, 0.f, 0.f, 0.f};
#pragma unroll
    for (int s = 0; s < 8; ++s) {
      bf16x8 a[3], o[5];
#pragma unroll
      for (int i = 0; i < 3; ++i) { const unsigned b = wb[i][s >> 2] + (s & 3) * 2048; a[i] = cat(ldstr(b), ldstr(b + 1024)); }
#pragma unroll
      for (int pt = 0; pt < 5; ++pt) o[pt] = ldsr128(QAREA + (((s >> 2) * 5 + pt) * 4 + (s & 3)) * 1024 + lane * 16);
#pragma unroll
      for (int pt = 0; pt < 5; ++pt)
#pragma unroll
        for (int i = 0; i < 3; ++i) acc[i][pt] = MFMA(a[i], o[pt], acc[i][pt]);
    }
    const __amdgpu_buffer_rsrc_t rp = __builtin_amdgcn_make_buffer_rsrc(part, 0, PART_BYTES, 0x00020000);
    const unsigned fb = (60 + w * 7) * 1024;
#pragma unroll
    for (int pt = 0; pt < 5; ++pt) {
      u32x4 v = {cvtpk(acc[0][pt][0], acc[0][pt][1]), cvtpk(acc[0][pt][2], acc[0][pt][3]), cvtpk(acc[1][pt][0], acc[1][pt][1]), cvtpk(acc[1][pt][2], acc[1][pt][3])};
      __builtin_amdgcn_raw_buffer_store_b128(v, rp, lane * 16, fb + pt * 1024, 16);
    }
#pragma unroll
    for (int f = 0; f < 2; ++f) {
      u32x4 v = {cvtpk(acc[2][2 * f][0], acc[2][2 * f][1]), cvtpk(acc[2][2 * f][2], acc[2][2 * f][3]), cvtpk(acc[2][2 * f + 1][0], acc[2][2 * f + 1][1]), cvtpk(acc[2][2 * f + 1][2], acc[2][2 * f + 1][3])};
      __builtin_amdgcn_raw_buffer_store_b128(v, rp, lane * 16, fb + (5 + f) * 1024, 16);
    }
    u32x2 v2 = {cvtpk(acc[2][4][0], acc[2][4][1]), cvtpk(acc[2][4][2], acc[2][4][3])};
    __builtin_amdgcn_raw_buffer_store_b64(v2, rp, lane * 8, 88 * 1024 + w * 512, 16);
  }
}

DI void helper_wave(const unsigned j, const unsigned lane, const unsigned b, const unsigned kvh, const char* x, const char* kc, const char* vc, const char* wq, const char* wo, char* part, unsigned long long* stamps) {
  const __amdgpu_buffer_rsrc_t rx = __builtin_amdgcn_make_buffer_rsrc((void*)(x + (size_t)b * 92160), 0, 92160, 0x00020000);
  const __amdgpu_buffer_rsrc_t rk = __builtin_amdgcn_make_buffer_rsrc((void*)(kc + (size_t)(b * 16 + kvh) * 262144), 0, 262144, 0x00020000);
  const __amdgpu_buffer_rsrc_t rv = __builtin_amdgcn_make_buffer_rsrc((void*)(vc + (size_t)(b * 16 + kvh) * 262144), 0, 262144, 0x00020000);
  const __amdgpu_buffer_rsrc_t rq = __builtin_amdgcn_make_buffer_rsrc((void*)(wq + (size_t)kvh * 512), 0, 576 * 8192 - kvh * 512, 0x00020000);
  const __amdgpu_buffer_rsrc_t ro = __builtin_amdgcn_make_buffer_rsrc((void*)(wo + (size_t)kvh * 256 * 1152), 0, 256 * 1152, 0x00020000);
  const unsigned cb = lane >> 5, row = (lane >> 2) & 7, c4 = lane & 3, r16 = lane >> 2;
  const unsigned vk = (8 * j + row) * 256 + cb * 64 + 16 * (c4 ^ swk(2 * (j & 1) + (row >> 2)));
  const unsigned vv = (8 * j + row) * 256 + cb * 64 + 16 * (c4 ^ (2 * (row >> 2)));
  const unsigned vq = (8 * j + row) * 8192 + cb * 64 + 16 * (c4 ^ (2 * (j & 1)));
  const unsigned vx = (16 * j + r16) * 1152 + 16 * (c4 ^ swk(r16 >> 2));
  const unsigned vx4 = (64 + 4 * j + (r16 & 3)) * 1152 + 16 * (c4 ^ swk(j));
  const unsigned vo = r16 * 1152 + 16 * (c4 ^ (2 * ((r16 >> 2) & 1)));
  auto dma_k = [&](int t) { dma<KV_NT>(KRING + (t % 6) * 8192 + j * 2048, vk, rk, t * 8192); dma<KV_NT>(KRING + (t % 6) * 8192 + j * 2048 + 1024, vk, rk, t * 8192 + 128); };
  auto dma_v = [&](int t) { dma<KV_NT>(VRING + (t % 6) * 8192 + j * 2048, vv, rv, t * 8192); dma<KV_NT>(VRING + (t % 6) * 8192 + j * 2048 + 1024, vv, rv, t * 8192 + 128); };
  unsigned ksn = (b * 9 / 8 + 9 * (kvh & 1)) % 18;
  auto dma_w = [&](int i) {
    const unsigned slot = WRING + (i % 3) * WSLOT;
    dma<false>(slot + XOFF + j * 1024, vx, rx, ksn * 64);
    if (lane < 16) dma<false>(slot + XOFF + 4096 + j * 256, vx4, rx, ksn * 64);
#pragma unroll
    for (int ns = 0; ns < 4; ++ns) dma<false>(slot + (ns * 4) * 1024 + j * 1024, vq, rq, ksn * 262144 + ns * 128);
    ksn = ksn == 17 ? 0 : ksn + 1;
  };
  auto dma_hu = [&](int pos) { if (pos == 0) dma_k(0); else if (pos & 1) dma_k((pos + 1) / 2); else dma_v(pos / 2 - 1); };
  asm volatile("s_nop 4" ::: "memory");
  dma_w(0); dma_w(1); dma_w(2);
  sfor<18>([&](auto ic) {
    constexpr int i = decltype(ic)::v;
    constexpr int n = i == 0 ? 12 : i == 1 ? 6 + 2 * hu_at(0) : i == 2 ? 2 * hu_at(0) + 6 + 2 * hu_at(1) : 2 * hu_at(i - 2) + (i + 1 < 18 ? 6 : 0) + 2 * hu_at(i - 1);
    wait_vm<n>();
    barrier();
    if constexpr (i >= 1 && i + 2 < 18) dma_w(i + 2);
    sfor<hu_at(i)>([&](auto ec) { dma_hu(hu_cum(i) - hu_at(i) + decltype(ec)::v); });
  });
  wait_vm<20>();
  barrier();
  bf16x8 W[3][2][8];
  const unsigned g = lane >> 4, q4 = (lane >> 2) & 3, p4 = lane & 3;
  const unsigned sa0 = QAREA + j * 10240 + (4 * g + q4) * 64 + 32 * (g & 1) + 8 * p4, sa1 = QAREA + j * 10240 + (4 * g + q4) * 64 + 32 * ((g & 1) ^ 1) + 8 * p4;
  auto dma_blk = [&](int idx) {
    const unsigned so = ((idx / 6) * 128 + 32 * j) * 1152 + (384 + 32 * (idx % 6)) * 2;
    dma<false>(blk_addr(idx) + j * 2048, vo, ro, so); dma<false>(blk_addr(idx) + j * 2048 + 1024, vo, ro, so + 16 * 1152);
  };
  sfor<32>([&](auto tc) {
    constexpr int t = decltype(tc)::v - 1;
    wait_vm<wait_b(t)>();
    barrier();
    if constexpr (t >= 0 && t < 24) {
      asm volatile("s_waitcnt lgkmcnt(0)" ::: "memory");
      const unsigned so = (32 * (t % 8)) * 1152 + (96 * j + 32 * (t / 8)) * 2;
      dma<false>(QAREA + j * 10240 + (t % 5) * 2048, vo, ro, so); dma<false>(QAREA + j * 10240 + (t % 5) * 2048 + 1024, vo, ro, so + 16 * 1152);
    }
    if constexpr (t == 24 || t == 25) dma_blk(t - 24);
    if constexpr (n_v(t) > 0) dma_v(t + 5);
    if constexpr (t + 7 <= 31) dma_k(t + 7);
    if constexpr (t >= 26) { dma_blk(2 * (t - 25)); dma_blk(2 * (t - 25) + 1); }
    if constexpr (t >= 4 && t < 28) {
      constexpr int u = t - 4;
      W[u / 8][0][u % 8] = cat(ldstr(sa0 + (u % 5) * 2048), ldstr(sa0 + (u % 5) * 2048 + 1024));
      W[u / 8][1][u % 8] = cat(ldstr(sa1 + (u % 5) * 2048), ldstr(sa1 + (u % 5) * 2048 + 1024));
      asm volatile("" : "+v"(W[u / 8][0][u % 8]), "+v"(W[u / 8][1][u % 8]));
    }
  });
  wait_vm<0>();
  barrier();
  const __amdgpu_buffer_rsrc_t rp = __builtin_amdgcn_make_buffer_rsrc(part, 0, PART_BYTES, 0x00020000);
#pragma unroll
  for (int p = 0; p < 3; ++p) {
    f32x4 acc[2][5];
#pragma unroll
    for (int pt = 0; pt < 5; ++pt) { acc[0][pt] = (f32x4){0.f, 0.f, 0.f, 0.f}; acc[1][pt] = (f32x4){0.f, 0.f, 0.f, 0.f}; }
#pragma unroll
    for (int s = 0; s < 8; ++s)
#pragma unroll
      for (int pt = 0; pt < 5; ++pt) {
        const bf16x8 o = ldsr128(QAREA + (((s >> 2) * 5 + pt) * 4 + (s & 3)) * 1024 + lane * 16);
        acc[0][pt] = MFMA(W[p][0][s], o, acc[0][pt]); acc[1][pt] = MFMA(W[p][1][s], o, acc[1][pt]);
      }
#pragma unroll
    for (int pt = 0; pt < 5; ++pt) {
      u32x4 v = {cvtpk(acc[0][pt][0], acc[0][pt][1]), cvtpk(acc[0][pt][2], acc[0][pt][3]), cvtpk(acc[1][pt][0], acc[1][pt][1]), cvtpk(acc[1][pt][2], acc[1][pt][3])};
      __builtin_amdgcn_raw_buffer_store_b128(v, rp, lane * 16, ((j * 3 + p) * 5 + pt) * 1024, 16);
    }
    asm volatile("" ::: "memory"); GAP();
  }
  if (j == 0) { STAMP(5); wait_vm<0>(); STAMP(6); }
}

__global__ __launch_bounds__(512, 2) void gqa_main(const char* __restrict__ x, const char* __restrict__ kc, const char* __restrict__ vc, const char* __restrict__ wq, const char* __restrict__ wo, char* __restrict__ ws) {
  extern __shared__ __attribute__((aligned(16))) char smem[];
  const unsigned lane = threadIdx.x & 63, w = rfl(threadIdx.x >> 6);
  const unsigned wg = blockIdx.x, kvh = 2 * (wg & 7) + ((wg >> 3) & 1), b = wg >> 4;
  char* part = ws + (size_t)(b * 16 + kvh) * PART_BYTES;
  unsigned long long* stamps = (unsigned long long*)(ws + STAMP_OFF);
  if (w == 0) STAMP(0);
  unsigned long long* cst = w == 0 ? stamps : stamps + 256 * NSTAMP * w;
  if (w >= 4) helper_wave(w - 4, lane, b, kvh, x, kc, vc, wq, wo, part, stamps);
  else if (w < 2) compute_wave<3>(w, lane, part, cst);
  else compute_wave<2>(w, lane, part, cst);
}

__global__ __launch_bounds__(384) void gqa_reduce(const char* __restrict__ ws, unsigned short* __restrict__ out) {
  const unsigned wg = blockIdx.x, tid = threadIdx.x, b = wg >> 4, c = wg & 15;
  if (tid >= 360) return;
  const unsigned G = 360 * c + tid;
  const u32x4* src = (const u32x4*)(ws + (size_t)b * 16 * PART_BYTES) + G;
  u32x4 v[16];
#pragma unroll
  for (int k = 0; k < 16; ++k) v[k] = src[k * (PART_BYTES / 16)];
  float a[8] = {0.f, 0.f, 0.f, 0.f, 0.f, 0.f, 0.f, 0.f};
#pragma unroll
  for (int k = 0; k < 16; ++k)
#pragma unroll
    for (int e = 0; e < 4; ++e) { a[2 * e] += __uint_as_float(v[k][e] << 16); a[2 * e + 1] += __uint_as_float(v[k][e] & 0xffff0000u); }
  const unsigned F = G >> 6, l = G & 63, g = l >> 4, r = l & 15;
  unsigned n0, n1, pos0, pos1;
  if (F < 60) { n0 = 32 * (F / 5) + 4 * g; n1 = n0 + 16; pos0 = pos1 = 16 * (F % 5) + r; }
  else if (F < 88) { const unsigned w = (F - 60) / 7, f = (F - 60) % 7;
    if (f < 5) { n0 = 384 + 48 * w + 4 * g; n1 = n0 + 16; pos0 = pos1 = 16 * f + r; }
    else { n0 = n1 = 384 + 48 * w + 32 + 4 * g; pos0 = 32 * (f - 5) + r; pos1 = pos0 + 16; } }
  else { const unsigned G2 = G - 88 * 64, w = G2 >> 5, l0 = 2 * (G2 & 31); n0 = n1 = 384 + 48 * w + 32 + 4 * (l0 >> 4); pos0 = 64 + (l0 & 15); pos1 = pos0 + 1; }
  *(u32x2*)(out + (size_t)(b * 80 + pos0) * 576 + n0) = (u32x2){cvtpk(a[0], a[1]), cvtpk(a[2], a[3])};
  *(u32x2*)(out + (size_t)(b * 80 + pos1) * 576 + n1) = (u32x2){cvtpk(a[4], a[5]), cvtpk(a[6], a[7])};
  if (tid == 0) ((unsigned long long*)(ws + STAMP_OFF))[blockIdx.x * NSTAMP + 7] = __builtin_amdgcn_s_memrealtime();
}

__global__ void probe_spin(const unsigned long long* ts) {
  unsigned long long t0 = __builtin_amdgcn_s_memrealtime();
  unsigned long long mn = ~0ull; for (int i = 0; i < 256; ++i) mn = ts[i * NSTAMP] < mn ? ts[i * NSTAMP] : mn; unsigned long long M = 0; for (int i = 0; i < 256; ++i) { unsigned long long d = ts[i * NSTAMP + 0] - mn; M = d > M ? d : M; }
  if (M > 20000) M = 20000;
  while (__builtin_amdgcn_s_memrealtime() - t0 < 100 * M + 20000) __builtin_amdgcn_s_sleep(8);
}

extern "C" void kernel_launch(void* const* d_in, const int* in_sizes, int n_in, void* d_out, int out_size, void* d_ws, size_t ws_size, hipStream_t stream) {
  (void)hipFuncSetAttribute((const void*)gqa_main, hipFuncAttributeMaxDynamicSharedMemorySize, (int)LDS_BYTES);
  gqa_main<<<256, 512, LDS_BYTES, stream>>>((const char*)d_in[0], (const char*)d_in[1], (const char*)d_in[2], (const char*)d_in[3], (const char*)d_in[4], (char*)d_ws);
  gqa_reduce<<<256, 384, 0, stream>>>((const char*)d_ws, (unsigned short*)d_out);
  probe_spin<<<1, 1, 0, stream>>>((const unsigned long long*)((char*)d_ws + STAMP_OFF));
}
```

```cpp
#define STAG 150
#define BO3_AT 40
#define SIG0 3
#define CODE_BYTES 28624
#include <hip/hip_runtime.h>
#include <hip/hip_bf16.h>

typedef __bf16 bf16x8 __attribute__((ext_vector_type(8)));
typedef __bf16 bf16x4 __attribute__((ext_vector_type(4)));
typedef __bf16 bf16x2 __attribute__((ext_vector_type(2)));
typedef float f32x4 __attribute__((ext_vector_type(4)));
typedef float f32x2 __attribute__((ext_vector_type(2)));
typedef unsigned u32x4 __attribute__((ext_vector_type(4)));
typedef unsigned u32x2 __attribute__((ext_vector_type(2)));
#define LAS __attribute__((address_space(3)))
#define DI __device__ __forceinline__
#define GAP() __builtin_amdgcn_sched_barrier(0)
#define MFMA(a, b, c) __builtin_amdgcn_mfma_f32_16x16x32_bf16((a), (b), (c), 0, 0, 0)
#define LGKM0() __builtin_amdgcn_s_waitcnt(0xC07F)

#define KV_NT 1
#define ROTC ((b * 9 / 16 + 4 * (kvh & 1)) % 9)
#define GROUPB (b & 1)
#define KVROT (((((b * 16 + kvh) * 2654435761u) >> 16) & 31) * 8192)
#define BURN 0
#define PACE 0
#define PACE0 100
#define PACEN 28
#define BO2_AT 4
#define POLL_SLEEP 4
#define KVSEL (b * 16 + kvh)
#define STAMP(i) do { } while (0)
#define ATTN_STAMP0
#define ATTN_STAMP1

#define ATTN_ASM \
  "v_mbcnt_lo_u32_b32 v247, -1, 0\n" \
  "v_mbcnt_hi_u32_b32 v247, -1, v247\n" \
  "s_lshr_b32 s45, %1, 1\n" \
  "s_mul_i32 s45, s45, 0x5000\n" \
  "s_and_b32 s44, %1, 1\n" \
  "s_lshl_b32 s46, s44, 13\n" \
  "s_add_u32 s45, s45, 98304\n" \
  "v_lshlrev_b32 v220, 4, v247\n" \
  "v_add_u32 v221, s45, v220\n" \
  "v_add_u32 v220, s46, v221\n" \
  "v_bfe_u32 v222, v247, 3, 1\n" \
  "v_lshlrev_b32 v244, 11, v222\n" \
  "v_and_b32 v222, 7, v247\n" \
  "v_lshl_or_b32 v244, v222, 6, v244\n" \
  "v_bfe_u32 v222, v247, 2, 2\n" \
  "v_sub_u32 v223, 4, v222\n" \
  "v_lshrrev_b32 v224, 4, v247\n" \
  "v_xor_b32 v223, v223, v224\n" \
  "v_and_b32 v223, 3, v223\n" \
  "v_lshl_or_b32 v244, v223, 4, v244\n" \
  "s_waitcnt lgkmcnt(0)\n" \
  "s_barrier\n" \
  "ds_read_b128 v[228:231], v244 offset:0\n" \
  "ds_read_b128 v[108:111], v220 offset:0\n" \
  "ds_read_b128 v[124:127], v220 offset:4096\n" \
  "ds_read_b128 v[140:143], v221 offset:16384\n" \
  "ds_read_b128 v[232:235], v244 offset:512\n" \
  "ds_read_b128 v[112:115], v220 offset:1024\n" \
  "ds_read_b128 v[128:131], v220 offset:5120\n" \
  "ds_read_b128 v[144:147], v221 offset:17408\n" \
  "ds_read_b128 v[236:239], v244 offset:1024\n" \
  "ds_read_b128 v[116:119], v220 offset:2048\n" \
  "ds_read_b128 v[132:135], v220 offset:6144\n" \
  "ds_read_b128 v[148:151], v221 offset:18432\n" \
  "ds_read_b128 v[240:243], v244 offset:1536\n" \
  "ds_read_b128 v[120:123], v220 offset:3072\n" \
  "ds_read_b128 v[136:139], v220 offset:7168\n" \
  "ds_read_b128 v[152:155], v221 offset:19456\n" \
  "v_mov_b64 v[0:1], 0\n" \
  "v_mov_b64 v[2:3], 0\n" \
  "v_mov_b64 v[4:5], 0\n" \
  "v_mov_b64 v[6:7], 0\n" \
  "v_mov_b64 v[8:9], 0\n" \
  "v_mov_b64 v[10:11], 0\n" \
  "v_mov_b64 v[12:13], 0\n" \
  "v_mov_b64 v[14:15], 0\n" \
  "v_mov_b64 v[16:17], 0\n" \
  "v_mov_b64 v[18:19], 0\n" \
  "v_mov_b64 v[20:21], 0\n" \
  "v_mov_b64 v[22:23], 0\n" \
  "v_mov_b64 v[24:25], 0\n" \
  "v_mov_b64 v[26:27], 0\n" \
  "v_mov_b64 v[28:29], 0\n" \
  "v_mov_b64 v[30:31], 0\n" \
  "v_mov_b64 v[32:33], 0\n" \
  "v_mov_b64 v[34:35], 0\n" \
  "v_mov_b64 v[36:37], 0\n" \
  "v_mov_b64 v[38:39], 0\n" \
  "v_mov_b64 v[40:41], 0\n" \
  "v_mov_b64 v[42:43], 0\n" \
  "v_mov_b64 v[44:45], 0\n" \
  "v_mov_b64 v[46:47], 0\n" \
  "v_mov_b64 v[48:49], 0\n" \
  "v_mov_b64 v[50:51], 0\n" \
  "v_mov_b64 v[52:53], 0\n" \
  "v_mov_b64 v[54:55], 0\n" \
  "v_mov_b64 v[56:57], 0\n" \
  "v_mov_b64 v[58:59], 0\n" \
  "v_mov_b64 v[60:61], 0\n" \
  "v_mov_b64 v[62:63], 0\n" \
  "v_mov_b64 v[64:65], 0\n" \
  "v_mov_b64 v[66:67], 0\n" \
  "v_mov_b64 v[68:69], 0\n" \
  "v_mov_b64 v[70:71], 0\n" \
  "v_mov_b64 v[72:73], 0\n" \
  "v_mov_b64 v[74:75], 0\n" \
  "v_mov_b64 v[76:77], 0\n" \
  "v_mov_b64 v[78:79], 0\n" \
  "v_mov_b64 v[80:81], 0\n" \
  "v_mov_b64 v[82:83], 0\n" \
  "v_mov_b64 v[84:85], 0\n" \
  "v_mov_b64 v[86:87], 0\n" \
  "v_mov_b64 v[88:89], 0\n" \
  "v_mov_b64 v[90:91], 0\n" \
  "v_mov_b64 v[92:93], 0\n" \
  "v_mov_b64 v[94:95], 0\n" \
  "v_mov_b64 v[96:97], 0\n" \
  "v_mov_b64 v[98:99], 0\n" \
  "v_mov_b64 v[100:101], 0\n" \
  "v_mov_b64 v[102:103], 0\n" \
  "v_mov_b64 v[104:105], 0\n" \
  "v_mov_b64 v[106:107], 0\n" \
  "s_cmp_lg_u32 %2, 0\n" \
  "s_cbranch_scc1 L_exact_%=\n" \
  "s_waitcnt lgkmcnt(14)\n" \
  "v_mfma_f32_16x16x32_bf16 v[168:171], v[228:231], v[108:111], 0\n" \
  "s_waitcnt lgkmcnt(13)\n" \
  "v_mfma_f32_16x16x32_bf16 v[172:175], v[228:231], v[124:127], 0\n" \
  "s_waitcnt lgkmcnt(12)\n" \
  "v_mfma_f32_16x16x32_bf16 v[176:179], v[228:231], v[140:143], 0\n" \
  "s_waitcnt lgkmcnt(10)\n" \
  "v_mfma_f32_16x16x32_bf16 v[168:171], v[232:235], v[112:115], v[168:171]\n" \
  "s_waitcnt lgkmcnt(9)\n" \
  "v_mfma_f32_16x16x32_bf16 v[172:175], v[232:235], v[128:131], v[172:175]\n" \
  "s_waitcnt lgkmcnt(8)\n" \
  "v_mfma_f32_16x16x32_bf16 v[176:179], v[232:235], v[144:147], v[176:179]\n" \
  "s_waitcnt lgkmcnt(6)\n" \
  "v_mfma_f32_16x16x32_bf16 v[168:171], v[236:239], v[116:119], v[168:171]\n" \
  "s_waitcnt lgkmcnt(5)\n" \
  "v_mfma_f32_16x16x32_bf16 v[172:175], v[236:239], v[132:135], v[172:175]\n" \
  "s_waitcnt lgkmcnt(4)\n" \
  "v_mfma_f32_16x16x32_bf16 v[176:179], v[236:239], v[148:151], v[176:179]\n" \
  "s_waitcnt lgkmcnt(2)\n" \
  "v_mfma_f32_16x16x32_bf16 v[168:171], v[240:243], v[120:123], v[168:171]\n" \
  "s_waitcnt lgkmcnt(1)\n" \
  "v_mfma_f32_16x16x32_bf16 v[172:175], v[240:243], v[136:139], v[172:175]\n" \
  "s_waitcnt lgkmcnt(0)\n" \
  "v_mfma_f32_16x16x32_bf16 v[176:179], v[240:243], v[152:155], v[176:179]\n" \
  "s_nop 7\n" \
  "v_max3_f32 v156, v168, v169, v170\n" \
  "v_max3_f32 v160, v172, v173, v174\n" \
  "v_max3_f32 v164, v176, v177, v178\n" \
  "v_max_f32 v156, v156, v171\n" \
  "v_max_f32 v160, v160, v175\n" \
  "v_max_f32 v164, v164, v179\n" \
  "v_mov_b32 v157, v156\n" \
  "v_mov_b32 v161, v160\n" \
  "v_mov_b32 v165, v164\n" \
  "s_nop 1\n" \
  "v_permlane16_swap_b32 v156, v157\n" \
  "v_permlane16_swap_b32 v160, v161\n" \
  "v_permlane16_swap_b32 v164, v165\n" \
  "s_nop 1\n" \
  "v_max_f32 v156, v156, v157\n" \
  "v_max_f32 v160, v160, v161\n" \
  "v_max_f32 v164, v164, v165\n" \
  "v_mov_b32 v157, v156\n" \
  "v_mov_b32 v161, v160\n" \
  "v_mov_b32 v165, v164\n" \
  "s_nop 1\n" \
  "v_permlane32_swap_b32 v156, v157\n" \
  "v_permlane32_swap_b32 v160, v161\n" \
  "v_permlane32_swap_b32 v164, v165\n" \
  "s_nop 1\n" \
  "v_max_f32 v156, v156, v157\n" \
  "v_max_f32 v160, v160, v161\n" \
  "v_max_f32 v164, v164, v165\n" \
  "s_branch L_refdone_%=\n" \
  "L_exact_%=:\n" \
  "s_mul_i32 s47, %1, 768\n" \
  "s_add_u32 s47, s47, 147456\n" \
  "v_lshl_add_u32 v222, v247, 2, s47\n" \
  "ds_read_b32 v156, v222 offset:0\n" \
  "ds_read_b32 v160, v222 offset:256\n" \
  "ds_read_b32 v164, v222 offset:512\n" \
  "s_waitcnt lgkmcnt(0)\n" \
  "L_refdone_%=:\n" \
  "v_add_u32 v244, s46, v244\n" \
  "ds_read_b128 v[208:211], v244 offset:0\n" \
  "ds_read_b128 v[212:215], v244 offset:4096\n" \
  "ds_read_b128 v[216:219], v244 offset:512\n" \
  "v_xor_b32 v156, 0x80000000, v156\n" \
  "v_mov_b32 v157, v156\n" \
  "v_mov_b32 v158, v156\n" \
  "v_mov_b32 v159, v156\n" \
  "v_xor_b32 v160, 0x80000000, v160\n" \
  "v_mov_b32 v161, v160\n" \
  "v_mov_b32 v162, v160\n" \
  "v_mov_b32 v163, v160\n" \
  "v_xor_b32 v164, 0x80000000, v164\n" \
  "v_mov_b32 v165, v164\n" \
  "v_mov_b32 v166, v164\n" \
  "v_mov_b32 v167, v164\n" \
  "v_lshrrev_b32 v222, 5, v247\n" \
  "v_lshlrev_b32 v245, 11, v222\n" \
  "v_bfe_u32 v222, v247, 2, 3\n" \
  "v_lshl_or_b32 v245, v222, 6, v245\n" \
  "v_bfe_u32 v222, v247, 4, 1\n" \
  "v_lshl_or_b32 v245, v222, 5, v245\n" \
  "v_and_b32 v222, 3, v247\n" \
  "v_lshl_or_b32 v245, v222, 3, v245\n" \
  "v_xor_b32 v246, 32, v245\n" \
  "s_add_u32 s42, s46, 0x4000\n" \
  "s_lshl_b32 s43, s46, 1\n" \
  "s_sub_u32 s43, s43, 0x2000\n" \
  "s_sub_u32 s45, s43, 0x10000\n" \
  "s_add_u32 s46, s43, 0x8000\n" \
  "s_mov_b32 s44, 0\n" \
  "s_mov_b32 s40, 16\n" \
  "v_mov_b32 v204, 0x3f803f80\n" \
  "v_mov_b32 v205, 0x3f803f80\n" \
  "v_mov_b32 v206, 0x3f803f80\n" \
  "v_mov_b32 v207, 0x3f803f80\n" \
  "v_mov_b64 v[192:193], 0\n" \
  "v_mov_b64 v[194:195], 0\n" \
  "v_mov_b64 v[196:197], 0\n" \
  "v_mov_b64 v[198:199], 0\n" \
  "v_mov_b64 v[184:185], 0\n" \
  "v_mov_b64 v[186:187], 0\n" \
  "s_waitcnt lgkmcnt(2)\n" \
  ATTN_STAMP0 \
  "L_loop_%=:\n" \
  "v_mfma_f32_16x16x32_bf16 v[168:171], v[208:211], v[108:111], v[156:159]\n" \
  "v_cvt_pk_bf16_f32 v198, v184, v185\n" \
  "v_cvt_pk_bf16_f32 v199, v186, v187\n" \
  "v_mfma_f32_16x16x32_bf16 v[172:175], v[208:211], v[124:127], v[160:163]\n" \
  "s_cmp_eq_u32 s44, 2\n" \
  "ds_read_b128 v[220:223], v244 offset:4608\n" \
  "v_mfma_f32_16x16x32_bf16 v[176:179], v[208:211], v[140:143], v[164:167]\n" \
  "s_waitcnt lgkmcnt(2)\n" \
  "ds_read_b128 v[208:211], v244 offset:1024\n" \
  "v_mfma_f32_16x16x32_bf16 v[180:183], v[212:215], v[108:111], v[156:159]\n" \
  "s_cselect_b32 s41, s45, s46\n" \
  "ds_read_b128 v[224:227], v244 offset:5120\n" \
  "v_mfma_f32_16x16x32_bf16 v[184:187], v[212:215], v[124:127], v[160:163]\n" \
  "s_cselect_b32 s44, -1, s44\n" \
  "ds_read_b128 v[228:231], v244 offset:1536\n" \
  "v_mfma_f32_16x16x32_bf16 v[188:191], v[212:215], v[140:143], v[164:167]\n" \
  "s_waitcnt lgkmcnt(4)\n" \
  "ds_read_b128 v[212:215], v244 offset:5632\n" \
  "v_mfma_f32_16x16x32_bf16 v[168:171], v[216:219], v[112:115], v[168:171]\n" \
  "s_add_u32 s44, s44, 1\n" \
  "ds_read_b64_tr_b16 v[232:233], v245 offset:0\n" \
  "v_xor_b32 v244, 0x2000, v244\n" \
  "v_mfma_f32_16x16x32_bf16 v[172:175], v[216:219], v[128:131], v[172:175]\n" \
  "ds_read_b64_tr_b16 v[234:235], v245 offset:4096\n" \
  "v_mfma_f32_16x16x32_bf16 v[176:179], v[216:219], v[144:147], v[176:179]\n" \
  "s_waitcnt lgkmcnt(6)\n" \
  "ds_read_b64_tr_b16 v[216:217], v246 offset:0\n" \
  "v_mfma_f32_16x16x32_bf16 v[180:183], v[220:223], v[112:115], v[180:183]\n" \
  "ds_read_b64_tr_b16 v[218:219], v246 offset:4096\n" \
  "v_mfma_f32_16x16x32_bf16 v[184:187], v[220:223], v[128:131], v[184:187]\n" \
  "ds_read_b64_tr_b16 v[236:237], v245 offset:512\n" \
  "v_mfma_f32_16x16x32_bf16 v[188:191], v[220:223], v[144:147], v[188:191]\n" \
  "s_waitcnt lgkmcnt(8)\n" \
  "ds_read_b64_tr_b16 v[238:239], v245 offset:4608\n" \
  "v_mfma_f32_16x16x32_bf16 v[168:171], v[208:211], v[116:119], v[168:171]\n" \
  "ds_read_b64_tr_b16 v[220:221], v246 offset:512\n" \
  "v_mfma_f32_16x16x32_bf16 v[172:175], v[208:211], v[132:135], v[172:175]\n" \
  "ds_read_b64_tr_b16 v[222:223], v246 offset:4608\n" \
  "v_mfma_f32_16x16x32_bf16 v[176:179], v[208:211], v[148:151], v[176:179]\n" \
  "s_waitcnt lgkmcnt(10)\n" \
  "ds_read_b64_tr_b16 v[208:209], v245 offset:1024\n" \
  "v_mfma_f32_16x16x32_bf16 v[180:183], v[224:227], v[116:119], v[180:183]\n" \
  "ds_read_b64_tr_b16 v[210:211], v245 offset:5120\n" \
  "v_mfma_f32_16x16x32_bf16 v[184:187], v[224:227], v[132:135], v[184:187]\n" \
  "ds_read_b64_tr_b16 v[240:241], v246 offset:1024\n" \
  "v_mfma_f32_16x16x32_bf16 v[188:191], v[224:227], v[148:151], v[188:191]\n" \
  "s_waitcnt lgkmcnt(12)\n" \
  "ds_read_b64_tr_b16 v[242:243], v246 offset:5120\n" \
  "v_mfma_f32_16x16x32_bf16 v[168:171], v[228:231], v[120:123], v[168:171]\n" \
  "ds_read_b64_tr_b16 v[224:225], v245 offset:1536\n" \
  "v_mfma_f32_16x16x32_bf16 v[172:175], v[228:231], v[136:139], v[172:175]\n" \
  "ds_read_b64_tr_b16 v[226:227], v245 offset:5632\n" \
  "v_mfma_f32_16x16x32_bf16 v[176:179], v[228:231], v[152:155], v[176:179]\n" \
  "s_waitcnt lgkmcnt(14)\n" \
  "ds_read_b64_tr_b16 v[228:229], v246 offset:1536\n" \
  "v_mfma_f32_16x16x32_bf16 v[180:183], v[212:215], v[120:123], v[180:183]\n" \
  "ds_read_b64_tr_b16 v[230:231], v246 offset:5632\n" \
  "v_mfma_f32_16x16x32_bf16 v[184:187], v[212:215], v[136:139], v[184:187]\n" \
  "v_exp_f32 v168, v168\n" \
  "v_mfma_f32_16x16x32_bf16 v[188:191], v[212:215], v[152:155], v[188:191]\n" \
  "v_exp_f32 v169, v169\n" \
  "v_mfma_f32_16x16x32_bf16 v[96:99], v[204:207], v[192:195], v[96:99]\n" \
  "v_exp_f32 v170, v170\n" \
  "v_mfma_f32_16x16x32_bf16 v[100:103], v[204:207], v[196:199], v[100:103]\n" \
  "s_waitcnt lgkmcnt(14)\n" \
  "v_exp_f32 v171, v171\n" \
  "v_mfma_f32_16x16x32_bf16 v[0:3], v[232:235], v[192:195], v[0:3]\n" \
  "v_exp_f32 v172, v172\n" \
  "v_mfma_f32_16x16x32_bf16 v[4:7], v[232:235], v[196:199], v[4:7]\n" \
  "s_waitcnt lgkmcnt(12)\n" \
  "ds_read_b128 v[212:215], v244 offset:0\n" \
  "v_exp_f32 v173, v173\n" \
  "v_mfma_f32_16x16x32_bf16 v[12:15], v[216:219], v[192:195], v[12:15]\n" \
  "v_exp_f32 v174, v174\n" \
  "v_mfma_f32_16x16x32_bf16 v[16:19], v[216:219], v[196:199], v[16:19]\n" \
  "s_waitcnt lgkmcnt(11)\n" \
  "v_exp_f32 v175, v175\n" \
  "v_mfma_f32_16x16x32_bf16 v[24:27], v[236:239], v[192:195], v[24:27]\n" \
  "ds_read_b128 v[216:219], v244 offset:4096\n" \
  "v_add_u32 v245, s42, v245\n" \
  "v_mfma_f32_16x16x32_bf16 v[28:31], v[236:239], v[196:199], v[28:31]\n" \
  "s_waitcnt lgkmcnt(10)\n" \
  "v_exp_f32 v180, v180\n" \
  "v_mfma_f32_16x16x32_bf16 v[36:39], v[220:223], v[192:195], v[36:39]\n" \
  "v_exp_f32 v181, v181\n" \
  "v_mfma_f32_16x16x32_bf16 v[40:43], v[220:223], v[196:199], v[40:43]\n" \
  "s_waitcnt lgkmcnt(8)\n" \
  "ds_read_b128 v[220:223], v244 offset:512\n" \
  "v_exp_f32 v182, v182\n" \
  "v_mfma_f32_16x16x32_bf16 v[48:51], v[208:211], v[192:195], v[48:51]\n" \
  "v_exp_f32 v183, v183\n" \
  "v_mfma_f32_16x16x32_bf16 v[52:55], v[208:211], v[196:199], v[52:55]\n" \
  "s_waitcnt lgkmcnt(7)\n" \
  "ds_read_b128 v[208:211], v244 offset:4608\n" \
  "v_add_u32 v246, s42, v246\n" \
  "v_mfma_f32_16x16x32_bf16 v[60:63], v[240:243], v[192:195], v[60:63]\n" \
  "v_exp_f32 v184, v184\n" \
  "v_mfma_f32_16x16x32_bf16 v[64:67], v[240:243], v[196:199], v[64:67]\n" \
  "s_waitcnt lgkmcnt(6)\n" \
  "v_exp_f32 v185, v185\n" \
  "v_mfma_f32_16x16x32_bf16 v[72:75], v[224:227], v[192:195], v[72:75]\n" \
  "s_mov_b32 s42, s41\n" \
  "v_exp_f32 v186, v186\n" \
  "v_mfma_f32_16x16x32_bf16 v[76:79], v[224:227], v[196:199], v[76:79]\n" \
  "s_waitcnt lgkmcnt(4)\n" \
  "v_exp_f32 v187, v187\n" \
  "v_mfma_f32_16x16x32_bf16 v[84:87], v[228:231], v[192:195], v[84:87]\n" \
  "v_cvt_pk_bf16_f32 v192, v168, v169\n" \
  "v_cvt_pk_bf16_f32 v193, v170, v171\n" \
  "v_mfma_f32_16x16x32_bf16 v[88:91], v[228:231], v[196:199], v[88:91]\n" \
  "s_waitcnt lgkmcnt(3)\n" \
  "v_cvt_pk_bf16_f32 v196, v172, v173\n" \
  "v_cvt_pk_bf16_f32 v197, v174, v175\n" \
  "v_mfma_f32_16x16x32_bf16 v[168:171], v[212:215], v[108:111], v[156:159]\n" \
  "v_cvt_pk_bf16_f32 v194, v180, v181\n" \
  "v_cvt_pk_bf16_f32 v195, v182, v183\n" \
  "v_mfma_f32_16x16x32_bf16 v[172:175], v[212:215], v[124:127], v[160:163]\n" \
  "s_waitcnt lgkmcnt(2)\n" \
  "v_cvt_pk_bf16_f32 v198, v184, v185\n" \
  "v_cvt_pk_bf16_f32 v199, v186, v187\n" \
  "v_mfma_f32_16x16x32_bf16 v[180:183], v[216:219], v[108:111], v[156:159]\n" \
  "ds_read_b128 v[212:215], v244 offset:1024\n" \
  "v_mfma_f32_16x16x32_bf16 v[184:187], v[216:219], v[124:127], v[160:163]\n" \
  "s_waitcnt lgkmcnt(2)\n" \
  "ds_read_b128 v[216:219], v244 offset:5120\n" \
  "v_mfma_f32_16x16x32_bf16 v[168:171], v[220:223], v[112:115], v[168:171]\n" \
  "ds_read_b128 v[224:227], v244 offset:1536\n" \
  "v_mfma_f32_16x16x32_bf16 v[172:175], v[220:223], v[128:131], v[172:175]\n" \
  "s_waitcnt lgkmcnt(3)\n" \
  "ds_read_b128 v[220:223], v244 offset:5632\n" \
  "v_mfma_f32_16x16x32_bf16 v[180:183], v[208:211], v[112:115], v[180:183]\n" \
  "s_barrier\n" \
  "v_exp_f32 v176, v176\n" \
  "v_mfma_f32_16x16x32_bf16 v[184:187], v[208:211], v[128:131], v[184:187]\n" \
  "s_waitcnt lgkmcnt(3)\n" \
  "v_exp_f32 v177, v177\n" \
  "v_mfma_f32_16x16x32_bf16 v[168:171], v[212:215], v[116:119], v[168:171]\n" \
  "v_exp_f32 v178, v178\n" \
  "v_mfma_f32_16x16x32_bf16 v[172:175], v[212:215], v[132:135], v[172:175]\n" \
  "s_waitcnt lgkmcnt(2)\n" \
  "v_exp_f32 v179, v179\n" \
  "v_mfma_f32_16x16x32_bf16 v[180:183], v[216:219], v[116:119], v[180:183]\n" \
  "ds_read_b64_tr_b16 v[208:209], v245 offset:0\n" \
  "v_cvt_pk_bf16_f32 v200, v176, v177\n" \
  "v_mfma_f32_16x16x32_bf16 v[184:187], v[216:219], v[132:135], v[184:187]\n" \
  "s_waitcnt lgkmcnt(2)\n" \
  "ds_read_b64_tr_b16 v[210:211], v245 offset:4096\n" \
  "v_cvt_pk_bf16_f32 v201, v178, v179\n" \
  "v_mfma_f32_16x16x32_bf16 v[168:171], v[224:227], v[120:123], v[168:171]\n" \
  "v_exp_f32 v188, v188\n" \
  "v_mfma_f32_16x16x32_bf16 v[172:175], v[224:227], v[136:139], v[172:175]\n" \
  "s_waitcnt lgkmcnt(2)\n" \
  "ds_read_b64_tr_b16 v[212:213], v246 offset:0\n" \
  "v_exp_f32 v189, v189\n" \
  "v_mfma_f32_16x16x32_bf16 v[180:183], v[220:223], v[120:123], v[180:183]\n" \
  "v_exp_f32 v190, v190\n" \
  "v_mfma_f32_16x16x32_bf16 v[184:187], v[220:223], v[136:139], v[184:187]\n" \
  "ds_read_b64_tr_b16 v[214:215], v246 offset:4096\n" \
  "v_exp_f32 v191, v191\n" \
  "v_mfma_f32_16x16x32_bf16 v[96:99], v[204:207], v[192:195], v[96:99]\n" \
  "ds_read_b64_tr_b16 v[216:217], v245 offset:512\n" \
  "v_cvt_pk_bf16_f32 v202, v188, v189\n" \
  "v_mfma_f32_16x16x32_bf16 v[100:103], v[204:207], v[196:199], v[100:103]\n" \
  "s_waitcnt lgkmcnt(3)\n" \
  "ds_read_b64_tr_b16 v[218:219], v245 offset:4608\n" \
  "v_cvt_pk_bf16_f32 v203, v190, v191\n" \
  "v_mfma_f32_16x16x32_bf16 v[0:3], v[208:211], v[192:195], v[0:3]\n" \
  "ds_read_b64_tr_b16 v[220:221], v246 offset:512\n" \
  "v_exp_f32 v168, v168\n" \
  "v_mfma_f32_16x16x32_bf16 v[4:7], v[208:211], v[196:199], v[4:7]\n" \
  "ds_read_b64_tr_b16 v[222:223], v246 offset:4608\n" \
  "v_mfma_f32_16x16x32_bf16 v[8:11], v[208:211], v[200:203], v[8:11]\n" \
  "v_exp_f32 v169, v169\n" \
  "v_mfma_f32_16x16x32_bf16 v[104:107], v[204:207], v[200:203], v[104:107]\n" \
  "s_waitcnt lgkmcnt(4)\n" \
  "ds_read_b64_tr_b16 v[208:209], v245 offset:1024\n" \
  "v_exp_f32 v170, v170\n" \
  "v_mfma_f32_16x16x32_bf16 v[12:15], v[212:215], v[192:195], v[12:15]\n" \
  "ds_read_b64_tr_b16 v[210:211], v245 offset:5120\n" \
  "v_add_u32 v244, s41, v244\n" \
  "v_mfma_f32_16x16x32_bf16 v[16:19], v[212:215], v[196:199], v[16:19]\n" \
  "v_exp_f32 v171, v171\n" \
  "v_mfma_f32_16x16x32_bf16 v[20:23], v[212:215], v[200:203], v[20:23]\n" \
  "s_waitcnt lgkmcnt(4)\n" \
  "ds_read_b64_tr_b16 v[224:225], v246 offset:1024\n" \
  "v_exp_f32 v172, v172\n" \
  "v_mfma_f32_16x16x32_bf16 v[24:27], v[216:219], v[192:195], v[24:27]\n" \
  "ds_read_b64_tr_b16 v[226:227], v246 offset:5120\n" \
  "v_mfma_f32_16x16x32_bf16 v[28:31], v[216:219], v[196:199], v[28:31]\n" \
  "v_exp_f32 v173, v173\n" \
  "v_mfma_f32_16x16x32_bf16 v[32:35], v[216:219], v[200:203], v[32:35]\n" \
  "s_waitcnt lgkmcnt(4)\n" \
  "ds_read_b64_tr_b16 v[228:229], v245 offset:1536\n" \
  "v_exp_f32 v174, v174\n" \
  "v_mfma_f32_16x16x32_bf16 v[36:39], v[220:223], v[192:195], v[36:39]\n" \
  "ds_read_b64_tr_b16 v[230:231], v245 offset:5632\n" \
  "v_mfma_f32_16x16x32_bf16 v[40:43], v[220:223], v[196:199], v[40:43]\n" \
  "v_exp_f32 v175, v175\n" \
  "v_mfma_f32_16x16x32_bf16 v[44:47], v[220:223], v[200:203], v[44:47]\n" \
  "s_waitcnt lgkmcnt(4)\n" \
  "ds_read_b64_tr_b16 v[220:221], v246 offset:1536\n" \
  "v_exp_f32 v180, v180\n" \
  "v_mfma_f32_16x16x32_bf16 v[48:51], v[208:211], v[192:195], v[48:51]\n" \
  "ds_read_b64_tr_b16 v[222:223], v246 offset:5632\n" \
  "v_exp_f32 v181, v181\n" \
  "v_mfma_f32_16x16x32_bf16 v[52:55], v[208:211], v[196:199], v[52:55]\n" \
  "s_barrier\n" \
  "v_exp_f32 v182, v182\n" \
  "v_mfma_f32_16x16x32_bf16 v[56:59], v[208:211], v[200:203], v[56:59]\n" \
  "s_waitcnt lgkmcnt(4)\n" \
  "ds_read_b128 v[208:211], v244 offset:0\n" \
  "v_xor_b32 v245, 0x2000, v245\n" \
  "v_mfma_f32_16x16x32_bf16 v[60:63], v[224:227], v[192:195], v[60:63]\n" \
  "v_exp_f32 v183, v183\n" \
  "v_mfma_f32_16x16x32_bf16 v[64:67], v[224:227], v[196:199], v[64:67]\n" \
  "ds_read_b128 v[212:215], v244 offset:4096\n" \
  "v_xor_b32 v246, 0x2000, v246\n" \
  "v_mfma_f32_16x16x32_bf16 v[68:71], v[224:227], v[200:203], v[68:71]\n" \
  "s_waitcnt lgkmcnt(4)\n" \
  "v_exp_f32 v184, v184\n" \
  "v_mfma_f32_16x16x32_bf16 v[72:75], v[228:231], v[192:195], v[72:75]\n" \
  "ds_read_b128 v[216:219], v244 offset:512\n" \
  "v_exp_f32 v185, v185\n" \
  "v_mfma_f32_16x16x32_bf16 v[76:79], v[228:231], v[196:199], v[76:79]\n" \
  "s_sub_u32 s40, s40, 1\n" \
  "v_exp_f32 v186, v186\n" \
  "v_mfma_f32_16x16x32_bf16 v[80:83], v[228:231], v[200:203], v[80:83]\n" \
  "s_waitcnt lgkmcnt(3)\n" \
  "v_exp_f32 v187, v187\n" \
  "v_mfma_f32_16x16x32_bf16 v[84:87], v[220:223], v[192:195], v[84:87]\n" \
  "s_cmp_lg_u32 s40, 0\n" \
  "v_cvt_pk_bf16_f32 v192, v168, v169\n" \
  "v_cvt_pk_bf16_f32 v193, v170, v171\n" \
  "v_mfma_f32_16x16x32_bf16 v[88:91], v[220:223], v[196:199], v[88:91]\n" \
  "v_cvt_pk_bf16_f32 v196, v172, v173\n" \
  "v_cvt_pk_bf16_f32 v197, v174, v175\n" \
  "v_mfma_f32_16x16x32_bf16 v[92:95], v[220:223], v[200:203], v[92:95]\n" \
  "s_waitcnt lgkmcnt(2)\n" \
  "v_cvt_pk_bf16_f32 v194, v180, v181\n" \
  "v_cvt_pk_bf16_f32 v195, v182, v183\n" \
  "s_cbranch_scc1 L_loop_%=\n" \
  ATTN_STAMP1 \
  "s_lshr_b32 s47, %1, 1\n" \
  "s_mul_i32 s47, s47, 0x5000\n" \
  "s_add_u32 s47, s47, 98304\n" \
  "s_lshl_b32 s45, %1, 2\n" \
  "s_add_u32 s45, s45, 8192\n" \
  "s_bitcmp1_b32 %1, 0\n" \
  "s_cbranch_scc1 L_odd_%=\n" \
  "ds_read_b64_tr_b16 v[108:109], v245 offset:0\n" \
  "ds_read_b64_tr_b16 v[110:111], v245 offset:4096\n" \
  "ds_read_b64_tr_b16 v[112:113], v246 offset:0\n" \
  "ds_read_b64_tr_b16 v[114:115], v246 offset:4096\n" \
  "ds_read_b64_tr_b16 v[116:117], v245 offset:512\n" \
  "ds_read_b64_tr_b16 v[118:119], v245 offset:4608\n" \
  "ds_read_b64_tr_b16 v[120:121], v246 offset:512\n" \
  "ds_read_b64_tr_b16 v[122:123], v246 offset:4608\n" \
  "ds_read_b64_tr_b16 v[124:125], v245 offset:1024\n" \
  "ds_read_b64_tr_b16 v[126:127], v245 offset:5120\n" \
  "ds_read_b64_tr_b16 v[128:129], v246 offset:1024\n" \
  "ds_read_b64_tr_b16 v[130:131], v246 offset:5120\n" \
  "ds_read_b64_tr_b16 v[132:133], v245 offset:1536\n" \
  "ds_read_b64_tr_b16 v[134:135], v245 offset:5632\n" \
  "ds_read_b64_tr_b16 v[136:137], v246 offset:1536\n" \
  "ds_read_b64_tr_b16 v[138:139], v246 offset:5632\n" \
  "v_cvt_pk_bf16_f32 v198, v184, v185\n" \
  "v_cvt_pk_bf16_f32 v199, v186, v187\n" \
  "v_lshl_add_u32 v220, v247, 4, s47\n" \
  "v_mfma_f32_16x16x32_bf16 v[96:99], v[204:207], v[192:195], v[96:99]\n" \
  "v_mov_b32 v221, s45\n" \
  "v_mov_b32 v223, 0x5f800000\n" \
  "v_mfma_f32_16x16x32_bf16 v[100:103], v[204:207], v[196:199], v[100:103]\n" \
  "s_waitcnt lgkmcnt(14)\n" \
  "v_mfma_f32_16x16x32_bf16 v[0:3], v[108:111], v[192:195], v[0:3]\n" \
  "s_waitcnt lgkmcnt(12)\n" \
  "ds_write_b128 v220, v[56:59] offset:12288\n" \
  "v_mfma_f32_16x16x32_bf16 v[12:15], v[112:115], v[192:195], v[12:15]\n" \
  "s_waitcnt lgkmcnt(11)\n" \
  "ds_write_b128 v220, v[68:71] offset:13312\n" \
  "v_mfma_f32_16x16x32_bf16 v[24:27], v[116:119], v[192:195], v[24:27]\n" \
  "s_waitcnt lgkmcnt(10)\n" \
  "ds_write_b128 v220, v[80:83] offset:14336\n" \
  "v_mfma_f32_16x16x32_bf16 v[36:39], v[120:123], v[192:195], v[36:39]\n" \
  "s_waitcnt lgkmcnt(9)\n" \
  "ds_write_b128 v220, v[92:95] offset:15360\n" \
  "v_mfma_f32_16x16x32_bf16 v[48:51], v[124:127], v[192:195], v[48:51]\n" \
  "s_waitcnt lgkmcnt(8)\n" \
  "ds_write_b128 v220, v[104:107] offset:18432\n" \
  "v_mfma_f32_16x16x32_bf16 v[60:63], v[128:131], v[192:195], v[60:63]\n" \
  "s_waitcnt lgkmcnt(7)\n" \
  "v_rcp_f32 v224, v96\n" \
  "v_mfma_f32_16x16x32_bf16 v[72:75], v[132:135], v[192:195], v[72:75]\n" \
  "s_waitcnt lgkmcnt(5)\n" \
  "v_add_f32 v222, v96, v100\n" \
  "v_add_f32 v222, v222, v104\n" \
  "v_mfma_f32_16x16x32_bf16 v[84:87], v[136:139], v[192:195], v[84:87]\n" \
  "v_cmp_nlt_f32 vcc, v222, v223\n" \
  "v_mfma_f32_16x16x32_bf16 v[4:7], v[108:111], v[196:199], v[4:7]\n" \
  "s_cmp_lg_u64 vcc, 0\n" \
  "v_mul_f32 v0, v224, v0\n" \
  "v_mul_f32 v1, v224, v1\n" \
  "v_mul_f32 v2, v224, v2\n" \
  "v_mfma_f32_16x16x32_bf16 v[16:19], v[112:115], v[196:199], v[16:19]\n" \
  "s_cselect_b32 s46, 1, 0\n" \
  "v_mul_f32 v3, v224, v3\n" \
  "v_cvt_pk_bf16_f32 v0, v0, v1\n" \
  "v_cvt_pk_bf16_f32 v1, v2, v3\n" \
  "v_mfma_f32_16x16x32_bf16 v[28:31], v[116:119], v[196:199], v[28:31]\n" \
  "v_mul_f32 v12, v224, v12\n" \
  "v_mul_f32 v13, v224, v13\n" \
  "v_mul_f32 v14, v224, v14\n" \
  "v_mfma_f32_16x16x32_bf16 v[40:43], v[120:123], v[196:199], v[40:43]\n" \
  "v_mul_f32 v15, v224, v15\n" \
  "v_cvt_pk_bf16_f32 v2, v12, v13\n" \
  "v_cvt_pk_bf16_f32 v3, v14, v15\n" \
  "v_mfma_f32_16x16x32_bf16 v[52:55], v[124:127], v[196:199], v[52:55]\n" \
  "ds_write_b128 v220, v[0:3] offset:0\n" \
  "v_mul_f32 v24, v224, v24\n" \
  "v_mul_f32 v25, v224, v25\n" \
  "v_mfma_f32_16x16x32_bf16 v[64:67], v[128:131], v[196:199], v[64:67]\n" \
  "v_mul_f32 v26, v224, v26\n" \
  "v_mul_f32 v27, v224, v27\n" \
  "v_cvt_pk_bf16_f32 v24, v24, v25\n" \
  "v_mfma_f32_16x16x32_bf16 v[76:79], v[132:135], v[196:199], v[76:79]\n" \
  "v_cvt_pk_bf16_f32 v25, v26, v27\n" \
  "v_mul_f32 v36, v224, v36\n" \
  "v_mul_f32 v37, v224, v37\n" \
  "v_mfma_f32_16x16x32_bf16 v[88:91], v[136:139], v[196:199], v[88:91]\n" \
  "v_mul_f32 v38, v224, v38\n" \
  "v_mul_f32 v39, v224, v39\n" \
  "v_cvt_pk_bf16_f32 v26, v36, v37\n" \
  "v_mov_b32 v222, s46\n" \
  "ds_write_b32 v221, v222\n" \
  "v_cvt_pk_bf16_f32 v27, v38, v39\n" \
  "ds_write_b128 v220, v[24:27] offset:1024\n" \
  "v_mul_f32 v48, v224, v48\n" \
  "v_mul_f32 v49, v224, v49\n" \
  "v_mul_f32 v50, v224, v50\n" \
  "v_mul_f32 v51, v224, v51\n" \
  "v_cvt_pk_bf16_f32 v48, v48, v49\n" \
  "v_cvt_pk_bf16_f32 v49, v50, v51\n" \
  "v_mul_f32 v60, v224, v60\n" \
  "v_mul_f32 v61, v224, v61\n" \
  "v_mul_f32 v62, v224, v62\n" \
  "v_mul_f32 v63, v224, v63\n" \
  "v_cvt_pk_bf16_f32 v50, v60, v61\n" \
  "v_cvt_pk_bf16_f32 v51, v62, v63\n" \
  "ds_write_b128 v220, v[48:51] offset:2048\n" \
  "v_mul_f32 v72, v224, v72\n" \
  "v_mul_f32 v73, v224, v73\n" \
  "v_mul_f32 v74, v224, v74\n" \
  "v_mul_f32 v75, v224, v75\n" \
  "v_cvt_pk_bf16_f32 v72, v72, v73\n" \
  "v_cvt_pk_bf16_f32 v73, v74, v75\n" \
  "v_mul_f32 v84, v224, v84\n" \
  "v_mul_f32 v85, v224, v85\n" \
  "v_mul_f32 v86, v224, v86\n" \
  "v_mul_f32 v87, v224, v87\n" \
  "v_cvt_pk_bf16_f32 v74, v84, v85\n" \
  "v_cvt_pk_bf16_f32 v75, v86, v87\n" \
  "ds_write_b128 v220, v[72:75] offset:3072\n" \
  "v_rcp_f32 v225, v100\n" \
  "v_mov_b32 v221, 8192\n" \
  "s_waitcnt lgkmcnt(0)\n" \
  "s_barrier\n" \
  "ds_read_b128 v[108:111], v220 offset:4096\n" \
  "ds_read_b128 v[112:115], v220 offset:5120\n" \
  "ds_read_b128 v[116:119], v220 offset:6144\n" \
  "ds_read_b128 v[120:123], v220 offset:7168\n" \
  "ds_read_b128 v[124:127], v220 offset:16384\n" \
  "ds_read_b128 v[128:131], v221 offset:0\n" \
  "v_mul_f32 v4, v225, v4\n" \
  "v_mul_f32 v5, v225, v5\n" \
  "v_mul_f32 v6, v225, v6\n" \
  "v_mul_f32 v7, v225, v7\n" \
  "v_cvt_pk_bf16_f32 v4, v4, v5\n" \
  "v_cvt_pk_bf16_f32 v5, v6, v7\n" \
  "v_mul_f32 v16, v225, v16\n" \
  "v_mul_f32 v17, v225, v17\n" \
  "v_mul_f32 v18, v225, v18\n" \
  "v_mul_f32 v19, v225, v19\n" \
  "v_cvt_pk_bf16_f32 v6, v16, v17\n" \
  "v_cvt_pk_bf16_f32 v7, v18, v19\n" \
  "s_waitcnt lgkmcnt(0)\n" \
  "ds_write_b128 v220, v[4:7] offset:4096\n" \
  "v_mul_f32 v28, v225, v28\n" \
  "v_mul_f32 v29, v225, v29\n" \
  "v_mul_f32 v30, v225, v30\n" \
  "v_mul_f32 v31, v225, v31\n" \
  "v_cvt_pk_bf16_f32 v28, v28, v29\n" \
  "v_cvt_pk_bf16_f32 v29, v30, v31\n" \
  "v_mul_f32 v40, v225, v40\n" \
  "v_mul_f32 v41, v225, v41\n" \
  "v_mul_f32 v42, v225, v42\n" \
  "v_mul_f32 v43, v225, v43\n" \
  "v_cvt_pk_bf16_f32 v30, v40, v41\n" \
  "v_cvt_pk_bf16_f32 v31, v42, v43\n" \
  "ds_write_b128 v220, v[28:31] offset:5120\n" \
  "v_mul_f32 v52, v225, v52\n" \
  "v_mul_f32 v53, v225, v53\n" \
  "v_mul_f32 v54, v225, v54\n" \
  "v_mul_f32 v55, v225, v55\n" \
  "v_cvt_pk_bf16_f32 v52, v52, v53\n" \
  "v_cvt_pk_bf16_f32 v53, v54, v55\n" \
  "v_mul_f32 v64, v225, v64\n" \
  "v_mul_f32 v65, v225, v65\n" \
  "v_mul_f32 v66, v225, v66\n" \
  "v_mul_f32 v67, v225, v67\n" \
  "v_cvt_pk_bf16_f32 v54, v64, v65\n" \
  "v_cvt_pk_bf16_f32 v55, v66, v67\n" \
  "ds_write_b128 v220, v[52:55] offset:6144\n" \
  "v_mul_f32 v76, v225, v76\n" \
  "v_mul_f32 v77, v225, v77\n" \
  "v_mul_f32 v78, v225, v78\n" \
  "v_mul_f32 v79, v225, v79\n" \
  "v_cvt_pk_bf16_f32 v76, v76, v77\n" \
  "v_cvt_pk_bf16_f32 v77, v78, v79\n" \
  "v_mul_f32 v88, v225, v88\n" \
  "v_mul_f32 v89, v225, v89\n" \
  "v_mul_f32 v90, v225, v90\n" \
  "v_mul_f32 v91, v225, v91\n" \
  "v_cvt_pk_bf16_f32 v78, v88, v89\n" \
  "v_cvt_pk_bf16_f32 v79, v90, v91\n" \
  "ds_write_b128 v220, v[76:79] offset:7168\n" \
  "v_add_f32 v104, v104, v124\n" \
  "v_rcp_f32 v226, v104\n" \
  "v_add_f32 v8, v8, v108\n" \
  "v_add_f32 v9, v9, v109\n" \
  "v_add_f32 v10, v10, v110\n" \
  "v_add_f32 v11, v11, v111\n" \
  "v_add_f32 v20, v20, v112\n" \
  "v_add_f32 v21, v21, v113\n" \
  "v_add_f32 v22, v22, v114\n" \
  "v_add_f32 v23, v23, v115\n" \
  "v_add_f32 v32, v32, v116\n" \
  "v_add_f32 v33, v33, v117\n" \
  "v_add_f32 v34, v34, v118\n" \
  "v_add_f32 v35, v35, v119\n" \
  "v_add_f32 v44, v44, v120\n" \
  "v_add_f32 v45, v45, v121\n" \
  "v_add_f32 v46, v46, v122\n" \
  "v_add_f32 v47, v47, v123\n" \
  "v_mul_f32 v8, v226, v8\n" \
  "v_mul_f32 v9, v226, v9\n" \
  "v_mul_f32 v10, v226, v10\n" \
  "v_mul_f32 v11, v226, v11\n" \
  "v_cvt_pk_bf16_f32 v8, v8, v9\n" \
  "v_cvt_pk_bf16_f32 v9, v10, v11\n" \
  "v_mul_f32 v20, v226, v20\n" \
  "v_mul_f32 v21, v226, v21\n" \
  "v_mul_f32 v22, v226, v22\n" \
  "v_mul_f32 v23, v226, v23\n" \
  "v_cvt_pk_bf16_f32 v10, v20, v21\n" \
  "v_cvt_pk_bf16_f32 v11, v22, v23\n" \
  "ds_write_b128 v220, v[8:11] offset:16384\n" \
  "v_mul_f32 v32, v226, v32\n" \
  "v_mul_f32 v33, v226, v33\n" \
  "v_mul_f32 v34, v226, v34\n" \
  "v_mul_f32 v35, v226, v35\n" \
  "v_cvt_pk_bf16_f32 v32, v32, v33\n" \
  "v_cvt_pk_bf16_f32 v33, v34, v35\n" \
  "v_mul_f32 v44, v226, v44\n" \
  "v_mul_f32 v45, v226, v45\n" \
  "v_mul_f32 v46, v226, v46\n" \
  "v_mul_f32 v47, v226, v47\n" \
  "v_cvt_pk_bf16_f32 v34, v44, v45\n" \
  "v_cvt_pk_bf16_f32 v35, v46, v47\n" \
  "ds_write_b128 v220, v[32:35] offset:17408\n" \
  "s_branch L_done_%=\n" \
  "L_odd_%=:\n" \
  "ds_read_b64_tr_b16 v[108:109], v245 offset:0\n" \
  "ds_read_b64_tr_b16 v[110:111], v245 offset:4096\n" \
  "ds_read_b64_tr_b16 v[112:113], v246 offset:0\n" \
  "ds_read_b64_tr_b16 v[114:115], v246 offset:4096\n" \
  "ds_read_b64_tr_b16 v[116:117], v245 offset:512\n" \
  "ds_read_b64_tr_b16 v[118:119], v245 offset:4608\n" \
  "ds_read_b64_tr_b16 v[120:121], v246 offset:512\n" \
  "ds_read_b64_tr_b16 v[122:123], v246 offset:4608\n" \
  "ds_read_b64_tr_b16 v[124:125], v245 offset:1024\n" \
  "ds_read_b64_tr_b16 v[126:127], v245 offset:5120\n" \
  "ds_read_b64_tr_b16 v[128:129], v246 offset:1024\n" \
  "ds_read_b64_tr_b16 v[130:131], v246 offset:5120\n" \
  "ds_read_b64_tr_b16 v[132:133], v245 offset:1536\n" \
  "ds_read_b64_tr_b16 v[134:135], v245 offset:5632\n" \
  "ds_read_b64_tr_b16 v[136:137], v246 offset:1536\n" \
  "ds_read_b64_tr_b16 v[138:139], v246 offset:5632\n" \
  "v_cvt_pk_bf16_f32 v198, v184, v185\n" \
  "v_cvt_pk_bf16_f32 v199, v186, v187\n" \
  "v_lshl_add_u32 v220, v247, 4, s47\n" \
  "v_mfma_f32_16x16x32_bf16 v[96:99], v[204:207], v[192:195], v[96:99]\n" \
  "v_mov_b32 v221, s45\n" \
  "v_mov_b32 v223, 0x5f800000\n" \
  "v_mfma_f32_16x16x32_bf16 v[100:103], v[204:207], v[196:199], v[100:103]\n" \
  "s_waitcnt lgkmcnt(14)\n" \
  "v_mfma_f32_16x16x32_bf16 v[0:3], v[108:111], v[192:195], v[0:3]\n" \
  "s_waitcnt lgkmcnt(12)\n" \
  "ds_write_b128 v220, v[8:11] offset:4096\n" \
  "v_mfma_f32_16x16x32_bf16 v[12:15], v[112:115], v[192:195], v[12:15]\n" \
  "s_waitcnt lgkmcnt(11)\n" \
  "ds_write_b128 v220, v[20:23] offset:5120\n" \
  "v_mfma_f32_16x16x32_bf16 v[24:27], v[116:119], v[192:195], v[24:27]\n" \
  "s_waitcnt lgkmcnt(10)\n" \
  "ds_write_b128 v220, v[32:35] offset:6144\n" \
  "v_mfma_f32_16x16x32_bf16 v[36:39], v[120:123], v[192:195], v[36:39]\n" \
  "s_waitcnt lgkmcnt(9)\n" \
  "ds_write_b128 v220, v[44:47] offset:7168\n" \
  "v_mfma_f32_16x16x32_bf16 v[48:51], v[124:127], v[192:195], v[48:51]\n" \
  "s_waitcnt lgkmcnt(8)\n" \
  "ds_write_b128 v220, v[104:107] offset:16384\n" \
  "v_mfma_f32_16x16x32_bf16 v[60:63], v[128:131], v[192:195], v[60:63]\n" \
  "s_waitcnt lgkmcnt(7)\n" \
  "v_rcp_f32 v224, v96\n" \
  "v_mfma_f32_16x16x32_bf16 v[72:75], v[132:135], v[192:195], v[72:75]\n" \
  "s_waitcnt lgkmcnt(5)\n" \
  "v_add_f32 v222, v96, v100\n" \
  "v_add_f32 v222, v222, v104\n" \
  "v_mfma_f32_16x16x32_bf16 v[84:87], v[136:139], v[192:195], v[84:87]\n" \
  "v_cmp_nlt_f32 vcc, v222, v223\n" \
  "v_mfma_f32_16x16x32_bf16 v[4:7], v[108:111], v[196:199], v[4:7]\n" \
  "s_cmp_lg_u64 vcc, 0\n" \
  "v_mul_f32 v0, v224, v0\n" \
  "v_mul_f32 v1, v224, v1\n" \
  "v_mul_f32 v2, v224, v2\n" \
  "v_mfma_f32_16x16x32_bf16 v[16:19], v[112:115], v[196:199], v[16:19]\n" \
  "s_cselect_b32 s46, 1, 0\n" \
  "v_mul_f32 v3, v224, v3\n" \
  "v_cvt_pk_bf16_f32 v0, v0, v1\n" \
  "v_cvt_pk_bf16_f32 v1, v2, v3\n" \
  "v_mfma_f32_16x16x32_bf16 v[28:31], v[116:119], v[196:199], v[28:31]\n" \
  "v_mul_f32 v12, v224, v12\n" \
  "v_mul_f32 v13, v224, v13\n" \
  "v_mul_f32 v14, v224, v14\n" \
  "v_mfma_f32_16x16x32_bf16 v[40:43], v[120:123], v[196:199], v[40:43]\n" \
  "v_mul_f32 v15, v224, v15\n" \
  "v_cvt_pk_bf16_f32 v2, v12, v13\n" \
  "v_cvt_pk_bf16_f32 v3, v14, v15\n" \
  "v_mfma_f32_16x16x32_bf16 v[52:55], v[124:127], v[196:199], v[52:55]\n" \
  "ds_write_b128 v220, v[0:3] offset:8192\n" \
  "v_mul_f32 v24, v224, v24\n" \
  "v_mul_f32 v25, v224, v25\n" \
  "v_mfma_f32_16x16x32_bf16 v[64:67], v[128:131], v[196:199], v[64:67]\n" \
  "v_mul_f32 v26, v224, v26\n" \
  "v_mul_f32 v27, v224, v27\n" \
  "v_cvt_pk_bf16_f32 v24, v24, v25\n" \
  "v_mfma_f32_16x16x32_bf16 v[76:79], v[132:135], v[196:199], v[76:79]\n" \
  "v_cvt_pk_bf16_f32 v25, v26, v27\n" \
  "v_mul_f32 v36, v224, v36\n" \
  "v_mul_f32 v37, v224, v37\n" \
  "v_mfma_f32_16x16x32_bf16 v[88:91], v[136:139], v[196:199], v[88:91]\n" \
  "v_mul_f32 v38, v224, v38\n" \
  "v_mul_f32 v39, v224, v39\n" \
  "v_cvt_pk_bf16_f32 v26, v36, v37\n" \
  "v_mov_b32 v222, s46\n" \
  "ds_write_b32 v221, v222\n" \
  "v_cvt_pk_bf16_f32 v27, v38, v39\n" \
  "ds_write_b128 v220, v[24:27] offset:9216\n" \
  "v_mul_f32 v48, v224, v48\n" \
  "v_mul_f32 v49, v224, v49\n" \
  "v_mul_f32 v50, v224, v50\n" \
  "v_mul_f32 v51, v224, v51\n" \
  "v_cvt_pk_bf16_f32 v48, v48, v49\n" \
  "v_cvt_pk_bf16_f32 v49, v50, v51\n" \
  "v_mul_f32 v60, v224, v60\n" \
  "v_mul_f32 v61, v224, v61\n" \
  "v_mul_f32 v62, v224, v62\n" \
  "v_mul_f32 v63, v224, v63\n" \
  "v_cvt_pk_bf16_f32 v50, v60, v61\n" \
  "v_cvt_pk_bf16_f32 v51, v62, v63\n" \
  "ds_write_b128 v220, v[48:51] offset:10240\n" \
  "v_mul_f32 v72, v224, v72\n" \
  "v_mul_f32 v73, v224, v73\n" \
  "v_mul_f32 v74, v224, v74\n" \
  "v_mul_f32 v75, v224, v75\n" \
  "v_cvt_pk_bf16_f32 v72, v72, v73\n" \
  "v_cvt_pk_bf16_f32 v73, v74, v75\n" \
  "v_mul_f32 v84, v224, v84\n" \
  "v_mul_f32 v85, v224, v85\n" \
  "v_mul_f32 v86, v224, v86\n" \
  "v_mul_f32 v87, v224, v87\n" \
  "v_cvt_pk_bf16_f32 v74, v84, v85\n" \
  "v_cvt_pk_bf16_f32 v75, v86, v87\n" \
  "ds_write_b128 v220, v[72:75] offset:11264\n" \
  "v_rcp_f32 v225, v100\n" \
  "v_mov_b32 v221, 8192\n" \
  "s_waitcnt lgkmcnt(0)\n" \
  "s_barrier\n" \
  "ds_read_b128 v[108:111], v220 offset:12288\n" \
  "ds_read_b128 v[112:115], v220 offset:13312\n" \
  "ds_read_b128 v[116:119], v220 offset:14336\n" \
  "ds_read_b128 v[120:123], v220 offset:15360\n" \
  "ds_read_b128 v[124:127], v220 offset:18432\n" \
  "ds_read_b128 v[128:131], v221 offset:0\n" \
  "v_mul_f32 v4, v225, v4\n" \
  "v_mul_f32 v5, v225, v5\n" \
  "v_mul_f32 v6, v225, v6\n" \
  "v_mul_f32 v7, v225, v7\n" \
  "v_cvt_pk_bf16_f32 v4, v4, v5\n" \
  "v_cvt_pk_bf16_f32 v5, v6, v7\n" \
  "v_mul_f32 v16, v225, v16\n" \
  "v_mul_f32 v17, v225, v17\n" \
  "v_mul_f32 v18, v225, v18\n" \
  "v_mul_f32 v19, v225, v19\n" \
  "v_cvt_pk_bf16_f32 v6, v16, v17\n" \
  "v_cvt_pk_bf16_f32 v7, v18, v19\n" \
  "s_waitcnt lgkmcnt(0)\n" \
  "ds_write_b128 v220, v[4:7] offset:12288\n" \
  "v_mul_f32 v28, v225, v28\n" \
  "v_mul_f32 v29, v225, v29\n" \
  "v_mul_f32 v30, v225, v30\n" \
  "v_mul_f32 v31, v225, v31\n" \
  "v_cvt_pk_bf16_f32 v28, v28, v29\n" \
  "v_cvt_pk_bf16_f32 v29, v30, v31\n" \
  "v_mul_f32 v40, v225, v40\n" \
  "v_mul_f32 v41, v225, v41\n" \
  "v_mul_f32 v42, v225, v42\n" \
  "v_mul_f32 v43, v225, v43\n" \
  "v_cvt_pk_bf16_f32 v30, v40, v41\n" \
  "v_cvt_pk_bf16_f32 v31, v42, v43\n" \
  "ds_write_b128 v220, v[28:31] offset:13312\n" \
  "v_mul_f32 v52, v225, v52\n" \
  "v_mul_f32 v53, v225, v53\n" \
  "v_mul_f32 v54, v225, v54\n" \
  "v_mul_f32 v55, v225, v55\n" \
  "v_cvt_pk_bf16_f32 v52, v52, v53\n" \
  "v_cvt_pk_bf16_f32 v53, v54, v55\n" \
  "v_mul_f32 v64, v225, v64\n" \
  "v_mul_f32 v65, v225, v65\n" \
  "v_mul_f32 v66, v225, v66\n" \
  "v_mul_f32 v67, v225, v67\n" \
  "v_cvt_pk_bf16_f32 v54, v64, v65\n" \
  "v_cvt_pk_bf16_f32 v55, v66, v67\n" \
  "ds_write_b128 v220, v[52:55] offset:14336\n" \
  "v_mul_f32 v76, v225, v76\n" \
  "v_mul_f32 v77, v225, v77\n" \
  "v_mul_f32 v78, v225, v78\n" \
  "v_mul_f32 v79, v225, v79\n" \
  "v_cvt_pk_bf16_f32 v76, v76, v77\n" \
  "v_cvt_pk_bf16_f32 v77, v78, v79\n" \
  "v_mul_f32 v88, v225, v88\n" \
  "v_mul_f32 v89, v225, v89\n" \
  "v_mul_f32 v90, v225, v90\n" \
  "v_mul_f32 v91, v225, v91\n" \
  "v_cvt_pk_bf16_f32 v78, v88, v89\n" \
  "v_cvt_pk_bf16_f32 v79, v90, v91\n" \
  "ds_write_b128 v220, v[76:79] offset:15360\n" \
  "v_add_f32 v104, v104, v124\n" \
  "v_rcp_f32 v226, v104\n" \
  "v_add_f32 v56, v56, v108\n" \
  "v_add_f32 v57, v57, v109\n" \
  "v_add_f32 v58, v58, v110\n" \
  "v_add_f32 v59, v59, v111\n" \
  "v_add_f32 v68, v68, v112\n" \
  "v_add_f32 v69, v69, v113\n" \
  "v_add_f32 v70, v70, v114\n" \
  "v_add_f32 v71, v71, v115\n" \
  "v_add_f32 v80, v80, v116\n" \
  "v_add_f32 v81, v81, v117\n" \
  "v_add_f32 v82, v82, v118\n" \
  "v_add_f32 v83, v83, v119\n" \
  "v_add_f32 v92, v92, v120\n" \
  "v_add_f32 v93, v93, v121\n" \
  "v_add_f32 v94, v94, v122\n" \
  "v_add_f32 v95, v95, v123\n" \
  "v_mul_f32 v56, v226, v56\n" \
  "v_mul_f32 v57, v226, v57\n" \
  "v_mul_f32 v58, v226, v58\n" \
  "v_mul_f32 v59, v226, v59\n" \
  "v_cvt_pk_bf16_f32 v56, v56, v57\n" \
  "v_cvt_pk_bf16_f32 v57, v58, v59\n" \
  "v_mul_f32 v68, v226, v68\n" \
  "v_mul_f32 v69, v226, v69\n" \
  "v_mul_f32 v70, v226, v70\n" \
  "v_mul_f32 v71, v226, v71\n" \
  "v_cvt_pk_bf16_f32 v58, v68, v69\n" \
  "v_cvt_pk_bf16_f32 v59, v70, v71\n" \
  "ds_write_b128 v220, v[56:59] offset:18432\n" \
  "v_mul_f32 v80, v226, v80\n" \
  "v_mul_f32 v81, v226, v81\n" \
  "v_mul_f32 v82, v226, v82\n" \
  "v_mul_f32 v83, v226, v83\n" \
  "v_cvt_pk_bf16_f32 v80, v80, v81\n" \
  "v_cvt_pk_bf16_f32 v81, v82, v83\n" \
  "v_mul_f32 v92, v226, v92\n" \
  "v_mul_f32 v93, v226, v93\n" \
  "v_mul_f32 v94, v226, v94\n" \
  "v_mul_f32 v95, v226, v95\n" \
  "v_cvt_pk_bf16_f32 v82, v92, v93\n" \
  "v_cvt_pk_bf16_f32 v83, v94, v95\n" \
  "ds_write_b128 v220, v[80:83] offset:19456\n" \
  "L_done_%=:\n" \
  "v_or3_b32 v128, v128, v129, v130\n" \
  "v_or_b32 v128, v128, v131\n" \
  "s_waitcnt lgkmcnt(0)\n" \
  "v_readfirstlane_b32 %0, v128\n" \
  ""
#define ATTN_CLOB "v0", "v1", "v2", "v3", "v4", "v5", "v6", "v7", "v8", "v9", "v10", "v11", "v12", "v13", "v14", "v15", "v16", "v17", "v18", "v19", "v20", "v21", "v22", "v23", "v24", "v25", "v26", "v27", "v28", "v29", "v30", "v31", "v32", "v33", "v34", "v35", "v36", "v37", "v38", "v39", "v40", "v41", "v42", "v43", "v44", "v45", "v46", "v47", "v48", "v49", "v50", "v51", "v52", "v53", "v54", "v55", "v56", "v57", "v58", "v59", "v60", "v61", "v62", "v63", "v64", "v65", "v66", "v67", "v68", "v69", "v70", "v71", "v72", "v73", "v74", "v75", "v76", "v77", "v78", "v79", "v80", "v81", "v82", "v83", "v84", "v85", "v86", "v87", "v88", "v89", "v90", "v91", "v92", "v93", "v94", "v95", "v96", "v97", "v98", "v99", "v100", "v101", "v102", "v103", "v104", "v105", "v106", "v107", "v108", "v109", "v110", "v111", "v112", "v113", "v114", "v115", "v116", "v117", "v118", "v119", "v120", "v121", "v122", "v123", "v124", "v125", "v126", "v127", "v128", "v129", "v130", "v131", "v132", "v133", "v134", "v135", "v136", "v137", "v138", "v139", "v140", "v141", "v142", "v143", "v144", "v145", "v146", "v147", "v148", "v149", "v150", "v151", "v152", "v153", "v154", "v155", "v156", "v157", "v158", "v159", "v160", "v161", "v162", "v163", "v164", "v165", "v166", "v167", "v168", "v169", "v170", "v171", "v172", "v173", "v174", "v175", "v176", "v177", "v178", "v179", "v180", "v181", "v182", "v183", "v184", "v185", "v186", "v187", "v188", "v189", "v190", "v191", "v192", "v193", "v194", "v195", "v196", "v197", "v198", "v199", "v200", "v201", "v202", "v203", "v204", "v205", "v206", "v207", "v208", "v209", "v210", "v211", "v212", "v213", "v214", "v215", "v216", "v217", "v218", "v219", "v220", "v221", "v222", "v223", "v224", "v225", "v226", "v227", "v228", "v229", "v230", "v231", "v232", "v233", "v234", "v235", "v236", "v237", "v238", "v239", "v240", "v241", "v242", "v243", "v244", "v245", "v246", "v247", "s40", "s41", "s42", "s43", "s44", "s45", "s46", "s47", "s48", "s49", "s50", "s51", "s52", "s53", "s54", "s55", "vcc", "scc", "memory"
#define HELP_MODE 0
#define HELP_Q_A \
  H_CODE(); H_X(0); H_WQ(0); H_WQ(1); H_TAG(); \
  H_WAIT(4); H_BAR(); H_X(1); H_X(2); H_X(3); H_WQ(2); \
  H_WAIT(13); H_BAR(); H_WQ(3); H_WQ(4); \
  H_WAIT(8); H_BAR(); H_X(4); H_WQ(5); \
  H_WAIT(11); H_BAR(); H_WQ(6); \
  H_WAIT(11); H_BAR(); H_WQ(7); \
  H_WAIT(8); H_BAR(); H_WQ(8); \
  H_WAIT(8); H_BAR(); H_X(5); H_WQ(9); \
  H_WAIT(11); H_BAR(); H_WQ(10); \
  H_WAIT(11); H_BAR(); H_X(6); H_WQ(11); \
  H_WAIT(11); H_BAR(); H_WQ(12); \
  H_WAIT(11); H_BAR(); H_X(7); H_WQ(13); \
  H_WAIT(11); H_BAR(); H_WQ(14); \
  H_WAIT(11); H_BAR(); H_X(8); H_WQ(15); \
  H_WAIT(11); H_BAR(); H_WQ(16); \
  H_WAIT(11); H_BAR(); H_WQ(17); \
  H_WAIT(8); H_BAR(); H_ITEM(0); \
  H_WAIT(8); H_BAR(); H_ITEM(1); \
  H_WAIT(8); H_BAR(); H_BURN(96); \
  do {} while (0)
#define HELP_EARLY_A \
  do {} while (0)
#define HELP_REST \
  H_WAIT(4); H_BAR(); H_ITEM(2); H_BURN(48); H_PACE(0); \
  H_WAIT(4); H_BAR(); H_UNIT(0); H_ITEM(3); H_PACE(1); \
  H_WAIT(6); H_BAR(); H_UNIT(1); H_ITEM(4); H_PACE(2); \
  H_WAIT(6); H_BAR(); H_UNIT(2); H_ITEM(5); H_PACE(3); \
  H_WAIT(6); H_BAR(); H_UNIT(3); H_ITEM(6); H_PACE(4); \
  H_WAIT(6); H_BAR(); H_UNIT(4); H_ITEM(7); H_PARK(0); H_PACE(5); \
  H_WAIT(6); H_BAR(); H_UNIT(5); H_ITEM(8); H_PARK(1); H_PACE(6); \
  H_WAIT(6); H_BAR(); H_UNIT(6); H_ITEM(9); H_PARK(2); H_PACE(7); \
  H_WAIT(6); H_BAR(); H_UNIT(7); H_ITEM(10); H_PARK(3); H_PACE(8); \
  H_WAIT(6); H_BAR(); H_UNIT(8); H_ITEM(11); H_PARK(4); H_PACE(9); \
  H_WAIT(6); H_BAR(); H_UNIT(9); H_ITEM(12); H_PARK(5); H_PACE(10); \
  H_WAIT(6); H_BAR(); H_UNIT(10); H_ITEM(13); H_PARK(6); H_PACE(11); \
  H_WAIT(6); H_BAR(); H_UNIT(11); H_ITEM(14); H_PARK(7); H_PACE(12); \
  H_WAIT(6); H_BAR(); H_UNIT(12); H_ITEM(15); H_PARK(8); H_PACE(13); \
  H_WAIT(6); H_BAR(); H_UNIT(13); H_ITEM(16); H_PARK(9); H_PACE(14); \
  H_WAIT(6); H_BAR(); H_UNIT(14); H_ITEM(17); H_PARK(10); H_PACE(15); \
  H_WAIT(6); H_BAR(); H_UNIT(15); H_ITEM(18); H_PARK(11); H_PACE(16); \
  H_WAIT(6); H_BAR(); H_UNIT(16); H_ITEM(19); H_PARK(12); H_PACE(17); \
  H_WAIT(6); H_BAR(); H_UNIT(17); H_ITEM(20); H_PARK(13); H_PACE(18); \
  H_WAIT(6); H_BAR(); H_UNIT(18); H_ITEM(21); H_PARK(14); H_PACE(19); \
  H_WAIT(6); H_BAR(); H_UNIT(19); H_ITEM(22); H_PARK(15); H_PACE(20); \
  H_WAIT(6); H_BAR(); H_UNIT(20); H_ITEM(23); H_PARK(16); H_PACE(21); \
  H_WAIT(6); H_BAR(); H_UNIT(21); H_ITEM(24); H_PARK(17); H_PACE(22); \
  H_WAIT(6); H_BAR(); H_UNIT(22); H_ITEM(25); H_PARK(18); H_PACE(23); \
  H_WAIT(6); H_BAR(); H_UNIT(23); H_ITEM(26); H_PARK(19); H_PACE(24); \
  H_WAIT(6); H_BAR(); H_BLK(0); H_ITEM(27); H_PARK(20); H_PACE(25); \
  H_WAIT(6); H_BAR(); H_BLK(1); H_ITEM(28); H_PARK(21); H_PACE(26); \
  H_WAIT(6); H_BAR(); H_BLK(2); H_ITEM(29); H_PARK(22); H_PACE(27); \
  H_WAIT(6); H_BAR(); H_ITEM(30); H_PARK(23); H_PACE(28); \
  H_WAIT(4); H_BAR(); H_ITEM(31); H_PACE(29); \
  H_WAIT(4); H_BAR(); H_BLK(3); H_BLK(4); H_BLK(5); H_BLK(6); H_PACE(30); \
  H_WAIT(8); H_BAR(); H_BLK(7); H_BLK(8); H_BLK(9); H_BLK(10); H_PACE(31); \
  H_BAR(); H_BLK(11); \
  H_BAR(); \
  do {} while (0)

constexpr unsigned RING = 0;
constexpr unsigned XAREA = 57344;
constexpr unsigned SCRIB = 49152;
constexpr unsigned WQAREA = 98304;
constexpr unsigned QAREA = 98304;
constexpr unsigned SPARE = 139264;
constexpr unsigned MAREA = 147456;
constexpr unsigned FLAGS = 8192;
constexpr unsigned LDS_BYTES = 163840;
constexpr unsigned PART_BYTES = 92 * 1024;
constexpr unsigned FLG_OFF = 32u << 20;
constexpr unsigned EPO_OFF = 33u << 20;
constexpr unsigned SPIN_CAP = 1u << 20;

DI unsigned rfl(unsigned x) { return __builtin_amdgcn_readfirstlane(x); }
DI unsigned cvtpk(float lo, float hi) { f32x2 v = {lo, hi}; bf16x2 b = __builtin_convertvector(v, bf16x2); return __builtin_bit_cast(unsigned, b); }
DI bf16x8 ldsr128(unsigned a) { return *(const LAS bf16x8*)a; }
DI bf16x4 ldstr(unsigned a) { return __builtin_amdgcn_ds_read_tr16_b64_v4bf16((LAS bf16x4*)a); }
DI bf16x8 cat(bf16x4 a, bf16x4 b) { return __builtin_shufflevector(a, b, 0, 1, 2, 3, 4, 5, 6, 7); }
DI void barrier() { asm volatile("s_barrier" ::: "memory"); }
template <int N> DI void wait_vm() { asm volatile("s_waitcnt vmcnt(%0)" :: "i"(N) : "memory"); }
template <unsigned LI, unsigned SI> DI void dma(unsigned lb, unsigned voff, __amdgpu_buffer_rsrc_t rs) {
  unsigned t;
  asm volatile("s_add_u32 m0, %1, %4\n\ts_mov_b32 %0, %5\n\tbuffer_load_dwordx4 %2, %3, %0 offen lds" : "=&s"(t) : "s"(lb), "v"(voff), "s"(rs), "i"(LI), "i"(SI) : "scc", "memory");
}
template <unsigned LI, unsigned SI> DI void dma_s(unsigned lb, unsigned voff, __amdgpu_buffer_rsrc_t rs, unsigned sb) {
  unsigned t;
  asm volatile("s_add_u32 m0, %1, %4\n\ts_add_u32 %0, %6, %5\n\tbuffer_load_dwordx4 %2, %3, %0 offen lds" : "=&s"(t) : "s"(lb), "v"(voff), "s"(rs), "i"(LI), "i"(SI), "s"(sb) : "scc", "memory");
}
template <unsigned LI, unsigned SI> DI void dma_kv(unsigned lb, unsigned voff, __amdgpu_buffer_rsrc_t rs, unsigned rot) {
  unsigned t;
  asm volatile("s_add_u32 %0, %6, %5\n\ts_add_u32 m0, %1, %4\n\ts_and_b32 %0, %0, 0x3ffff\n\tbuffer_load_dwordx4 %2, %3, %0 offen nt lds" : "=&s"(t) : "s"(lb), "v"(voff), "s"(rs), "i"(LI), "i"(SI), "s"(rot) : "scc", "memory");
}
DI void touch(unsigned lds, unsigned voff, __amdgpu_buffer_rsrc_t rs) {
  asm volatile("s_mov_b32 m0, %0\n\ts_nop 0\n\tbuffer_load_dword %1, %2, 0 offen lds" :: "s"(lds), "v"(voff), "s"(rs) : "memory");
}
DI unsigned lane_id() { unsigned l; asm volatile("v_mbcnt_lo_u32_b32 %0, -1, 0\n\tv_mbcnt_hi_u32_b32 %0, -1, %0" : "=v"(l)); return l; }
DI unsigned swk(unsigned r4) { return (4u - r4) & 3u; }
template <int I> struct IC { static constexpr int v = I; };
template <int N, int I = 0, class F> DI void sfor(F&& f) { if constexpr (I < N) { f(IC<I>{}); sfor<N, I + 1>(f); } }
DI bool redo_flag() { const u32x4 f = *(const LAS u32x4*)FLAGS; return rfl(f[0] | f[1] | f[2] | f[3]) != 0; }

DI void q_project(const unsigned w, unsigned long long* stamps) {
  const unsigned lane = lane_id(), r = lane & 15, g = lane >> 4, q4 = (lane >> 2) & 3, p4 = lane & 3;
  f32x4 acc[4][5];
#pragma unroll
  for (int t = 0; t < 4; ++t)
#pragma unroll
    for (int pt = 0; pt < 5; ++pt) acc[t][pt] = (f32x4){0.f, 0.f, 0.f, 0.f};
  unsigned wa0 = WQAREA + w * 4096 + g * 1024 + q4 * 64 + 32 * (g & 1) + 8 * p4;
  unsigned wa1 = wa0 ^ 32;
  unsigned xa = XAREA + (r >> 3) * 1024 + (r & 7) * 64 + 16 * (g ^ swk(r >> 2));
  asm volatile("" : "+v"(wa0), "+v"(wa1), "+v"(xa));
  bf16x4 al[2][4], ah[2][4]; bf16x8 xf[2][5];
  auto rd = [&](auto ec, auto zc, const unsigned a0, const unsigned a1, const unsigned ax) {
    constexpr int e = decltype(ec)::v, z = decltype(zc)::v;
    if constexpr (e < 8) { constexpr int t = e >> 1; const unsigned a = ((t & 1) ? a1 : a0) + (t >> 1) * 512;
      if constexpr (e & 1) ah[z][t] = ldstr(a + 256); else al[z][t] = ldstr(a); }
    else xf[z][e - 8] = ldsr128(ax + (e - 8) * 2048);
  };
  unsigned wo_ = 32768, xo_ = 30720;
  unsigned a0 = wa0 + wo_, a1 = wa1 + wo_, ax = xa + xo_;
  barrier();
  STAMP(9);
  sfor<13>([&](auto ec) { rd(ec, IC<0>{}, a0, a1, ax); });
  GAP();
#pragma unroll 1
  for (int c = 0; c < 9; ++c) {
    LGKM0(); GAP();
    barrier();
    GAP();
    wo_ ^= 32768; xo_ = xo_ == 30720 ? 0 : xo_ + 10240;
    unsigned n0, n1, nx;
    sfor<20>([&](auto mc) {
      constexpr int m = decltype(mc)::v, pt = m >> 2, t = m & 3;
      acc[t][pt] = MFMA(cat(al[0][t], ah[0][t]), xf[0][pt], acc[t][pt]);
      GAP();
      if constexpr (m < 13) rd(IC<m>{}, IC<1>{}, a0 + 16384, a1 + 16384, ax + 512);
      if constexpr (m == 14) { n0 = wa0 + wo_; n1 = wa1 + wo_; asm volatile("" : "+v"(n0), "+v"(n1)); }
      if constexpr (m == 15) { nx = xa + xo_; asm volatile("" : "+v"(nx)); }
      GAP();
    });
    a0 = n0; a1 = n1; ax = nx;
    LGKM0(); GAP();
    if (c < 8) barrier();
    GAP();
    sfor<20>([&](auto mc) {
      constexpr int m = decltype(mc)::v, pt = m >> 2, t = m & 3;
      acc[t][pt] = MFMA(cat(al[1][t], ah[1][t]), xf[1][pt], acc[t][pt]);
      GAP();
      if constexpr (m < 13) rd(IC<m>{}, IC<0>{}, a0, a1, ax);
      GAP();
    });
  }
  STAMP(10);
  const float c = 0.12751743082459868f;
  const unsigned qa = QAREA + ((w >> 1) * 20 + 2 * (w & 1)) * 1024 + (16 * (g >> 1) + r) * 16 + 8 * (g & 1);
#pragma unroll
  for (int pt = 0; pt < 5; ++pt)
#pragma unroll
    for (int t = 0; t < 4; ++t) {
      const f32x4 v = acc[t][pt] * c;
      *(LAS u32x2*)(qa + pt * 4096 + (t >> 1) * 1024 + (t & 1) * 512) = (u32x2){cvtpk(v[0], v[1]), cvtpk(v[2], v[3])};
    }
}

DI void row_maxima(const unsigned w, const char* kp) {
  const unsigned lane = lane_id(), r = lane & 15, g = lane >> 4;
  bf16x8 Q[3][4];
#pragma unroll
  for (int T = 0; T < 3; ++T)
#pragma unroll
    for (int ks = 0; ks < 4; ++ks) Q[T][ks] = ldsr128(QAREA + ((w >> 1) * 5 + (T < 2 ? 2 * (w & 1) + T : 4)) * 4096 + ks * 1024 + lane * 16);
  const __amdgpu_buffer_rsrc_t rk = __builtin_amdgcn_make_buffer_rsrc((void*)kp, 0, 262144, 0x00020000);
  float m[3] = {-__builtin_inff(), -__builtin_inff(), -__builtin_inff()};
#pragma unroll 1
  for (int kt = 0; kt < 64; ++kt) {
    bf16x8 kf[4];
#pragma unroll
    for (int ks = 0; ks < 4; ++ks) kf[ks] = __builtin_bit_cast(bf16x8, __builtin_amdgcn_raw_buffer_load_b128(rk, r * 256 + g * 16 + ks * 64, kt * 4096, 0));
#pragma unroll
    for (int T = 0; T < 3; ++T) {
      f32x4 s = {0.f, 0.f, 0.f, 0.f};
#pragma unroll
      for (int ks = 0; ks < 4; ++ks) s = MFMA(kf[ks], Q[T][ks], s);
      m[T] = fmaxf(fmaxf(m[T], fmaxf(s[0], s[1])), fmaxf(s[2], s[3]));
    }
  }
#pragma unroll
  for (int T = 0; T < 3; ++T) {
    float x = m[T];
    { const auto y = __builtin_amdgcn_permlane16_swap(__float_as_uint(x), __float_as_uint(x), false, false); x = fmaxf(__uint_as_float(y[0]), __uint_as_float(y[1])); }
    { const auto y = __builtin_amdgcn_permlane32_swap(__float_as_uint(x), __float_as_uint(x), false, false); x = fmaxf(__uint_as_float(y[0]), __uint_as_float(y[1])); }
    *(LAS float*)(MAREA + w * 768 + T * 256 + lane * 4) = x;
  }
}

constexpr int ORD[5] = {0, 2, 1, 3, 4};
DI unsigned long long load_tag(const char* ws, const unsigned b) {
  unsigned long long t;
  asm volatile("s_load_dwordx2 %0, %1, 0x0\n\ts_waitcnt lgkmcnt(0)" : "=&s"(t) : "s"(ws + EPO_OFF + b * 128) : "memory");
  return t + 0x9E3779B97F4A7C15ull;
}
DI void store_wt(const u32x4 v, const __amdgpu_buffer_rsrc_t rs, const unsigned voff, const unsigned soff) {
  __builtin_amdgcn_raw_buffer_store_b128(v, rs, voff, soff, 16);
  GAP(); asm volatile("s_nop 1" ::: "memory"); GAP();
}
DI void signal(char* ws, const unsigned b, const unsigned kvh, const unsigned slot, const unsigned long long tag) {
  const __amdgpu_buffer_rsrc_t rf = __builtin_amdgcn_make_buffer_rsrc(ws + FLG_OFF, 0, 16 * 24 * 16 * 128, 0x00020000);
  const unsigned l = lane_id();
  __builtin_amdgcn_raw_buffer_store_b64((u32x2){(unsigned)tag, (unsigned)(tag >> 32)}, rf, l == 0 ? 0u : 0xfffffff0u, ((b * 24 + slot) * 16 + kvh) * 128, 16);
}

DI void compute_wave(const unsigned w, const unsigned b, const unsigned kvh, const char* kp, char* ws, char* out, const unsigned long long tag, unsigned long long* stamps) {
#pragma unroll 1
  for (unsigned pass = 0;; ++pass) {
    q_project(w, stamps);
    STAMP(1);
    if (pass) { asm volatile("s_waitcnt lgkmcnt(0)" ::: "memory"); barrier(); row_maxima(w, kp); }
    unsigned redo;
    asm volatile(ATTN_ASM : "=s"(redo) : "s"(w), "s"(pass) : ATTN_CLOB);
    barrier();
    if (pass || !redo) break;
  }
  STAMP(4);
  const unsigned lane = lane_id(), g = lane >> 4, q4 = (lane >> 2) & 3, p4 = lane & 3;
  {
    unsigned wb[3][3];
#pragma unroll
    for (int i = 0; i < 3; ++i) {
      const unsigned tau = 3 * w + i, li = tau >> 2, t4 = tau & 3;
      const unsigned la = (g >> 1) * 1024 + (t4 >> 1) * 512 + (4 * (g & 1) + q4) * 64 + 32 * ((t4 ^ g) & 1) + 8 * p4;
      wb[i][0] = SPARE + li * 8192 + la; wb[i][1] = 32768 + li * 8192 + la; wb[i][2] = (li < 2 ? 81920 + li * 8192 : 0) + la;
      asm volatile("" : "+v"(wb[i][0]), "+v"(wb[i][1]), "+v"(wb[i][2]));
    }
    unsigned oa = QAREA + lane * 16;
    asm volatile("" : "+v"(oa));
    bf16x8 A[3][8], of[4];
    f32x4 acc[5][3];
    const __amdgpu_buffer_rsrc_t rp = __builtin_amdgcn_make_buffer_rsrc(ws + (size_t)(b * 16 + kvh) * PART_BYTES, 0, PART_BYTES, 0x00020000);
    const unsigned fb = (60 + w * 8) * 1024;
    auto rdo = [&](auto nc) { constexpr int n = decltype(nc)::v, s = n & 7, pt = ORD[n >> 3]; of[n & 3] = ldsr128(oa + (((s >> 2) * 5 + pt) * 4 + (s & 3)) * 1024); };
    auto rda = [&](auto sc) { constexpr int s = decltype(sc)::v, rg = s >> 1;
      sfor<3>([&](auto ic) { constexpr int i = decltype(ic)::v; const unsigned a = wb[i][rg == 0 ? 0 : rg == 3 ? 2 : 1] + (rg == 2 ? 24576 : 0) + (s & 1) * 4096; A[i][s] = cat(ldstr(a), ldstr(a + 2048)); }); };
    auto put = [&](const f32x4 a, const f32x4 c, const int f) {
      const u32x4 v = {cvtpk(a[0], a[1]), cvtpk(a[2], a[3]), cvtpk(c[0], c[1]), cvtpk(c[2], c[3])};
      store_wt(v, rp, lane * 16, fb + f * 1024); };
    rda(IC<0>{}); rdo(IC<0>{}); rda(IC<1>{}); rdo(IC<1>{}); rdo(IC<2>{});
    GAP();
    sfor<40>([&](auto nc) {
      constexpr int n = decltype(nc)::v, r = n >> 3, s = n & 7;
      const f32x4 zero = {0.f, 0.f, 0.f, 0.f};
      acc[r][0] = MFMA(A[0][s], of[n & 3], s == 0 ? zero : acc[r][0]);
      GAP();
      if constexpr (n + 2 < 8) rda(IC<n + 2>{});
      if constexpr (n + 3 < 40) rdo(IC<n + 3>{});
      GAP();
      acc[r][1] = MFMA(A[1][s], of[n & 3], s == 0 ? zero : acc[r][1]);
      GAP();
      if constexpr (r > 0 && s == 1) put(acc[r - 1][0], acc[r - 1][1], r - 1);
      if constexpr (r > 0 && s == 3 && (r & 1) == 0) put(acc[r - 2][2], acc[r - 1][2], 4 + r / 2);
      GAP();
      acc[r][2] = MFMA(A[2][s], of[n & 3], s == 0 ? zero : acc[r][2]);
      GAP();
    });
    put(acc[4][0], acc[4][1], 4); put(acc[4][2], acc[4][2], 7);
    STAMP(8);
    wait_vm<0>();
    signal(ws, b, kvh, w, tag);
    if constexpr (BO3_AT >= 0) barrier();
  }
  const __amdgpu_buffer_rsrc_t rb = __builtin_amdgcn_make_buffer_rsrc(ws + (size_t)b * 16 * PART_BYTES, 0, 16 * PART_BYTES, 0x00020000);
  const __amdgpu_buffer_rsrc_t rf = __builtin_amdgcn_make_buffer_rsrc(ws + FLG_OFF, 0, 16 * 24 * 16 * 128, 0x00020000);
  const __amdgpu_buffer_rsrc_t rout = __builtin_amdgcn_make_buffer_rsrc(out + (size_t)b * 92160, 0, 92160, 0x00020000);
  unsigned F = w < 2 ? 60 + 16 * w + kvh : 16 * (w - 2) + kvh;
#pragma unroll 1
  for (;;) {
    unsigned slot, o0, o1;
    if (F < 60) { const unsigned rr = F / 12, j = (F % 12) / 3, p = F % 3; slot = 4 + (rr < SIG0 ? SIG0 : rr) * 4 + j; o0 = ((0x43120u >> (4 * rr)) & 15) * 16 * 1152 + (128 * p + 32 * j) * 2; o1 = o0 + 32; }
    else { const unsigned cw = (F - 60) >> 3, f = (F - 60) & 7; slot = cw;
      if (f < 5) { o0 = ((0x43120u >> (4 * f)) & 15) * 16 * 1152 + (384 + 48 * cw) * 2; o1 = o0 + 32; }
      else { o0 = ((0x43120u >> (8 * (f - 5))) & 15) * 16 * 1152 + (416 + 48 * cw) * 2; o1 = f < 7 ? o0 + 32 * 1152 : 0; } }
#pragma unroll 1
    for (unsigned spins = 0; spins < SPIN_CAP; ++spins) {
      asm volatile("" ::: "memory");
      const u32x2 fl = __builtin_amdgcn_raw_buffer_load_b64(rf, (lane & 15) * 128, (b * 24 + slot) * 2048, 16);
      if (__all(fl[0] == (unsigned)tag && fl[1] == (unsigned)(tag >> 32))) break;
      __builtin_amdgcn_s_sleep(POLL_SLEEP);
    }
    asm volatile("" ::: "memory");
    if (kvh == 0 && w == 2) __hip_atomic_store((__attribute__((address_space(1))) unsigned long long*)(ws + EPO_OFF + b * 128), tag, __ATOMIC_RELAXED, __HIP_MEMORY_SCOPE_AGENT);
    u32x4 v[16];
#pragma unroll
    for (int k = 0; k < 16; ++k) v[k] = __builtin_amdgcn_raw_buffer_load_b128(rb, lane * 16, k * PART_BYTES + F * 1024, 16);
    float a[8] = {0.f, 0.f, 0.f, 0.f, 0.f, 0.f, 0.f, 0.f};
#pragma unroll
    for (int k = 0; k < 16; ++k)
#pragma unroll
      for (int e = 0; e < 4; ++e) { a[2 * e] += __uint_as_float(v[k][e] << 16); a[2 * e + 1] += __uint_as_float(v[k][e] & 0xffff0000u); }
    const unsigned lo = (lane & 15) * 1152 + g * 8;
    __builtin_amdgcn_raw_buffer_store_b64((u32x2){cvtpk(a[0], a[1]), cvtpk(a[2], a[3])}, rout, lo, o0, 0);
    if (o1) __builtin_amdgcn_raw_buffer_store_b64((u32x2){cvtpk(a[4], a[5]), cvtpk(a[6], a[7])}, rout, lo, o1, 0);
    if (F < 60) break;
    F = 32 + 16 * w + kvh;
    if (F >= 60) break;
  }
}

DI void helper_wave(const unsigned j, const unsigned b, const unsigned kvh, const char* x, const char* kp, const char* vp, const char* wq, const char* wo, char* ws, const unsigned long long code0, unsigned long long* stamps) {
  unsigned long long tag = 0;
  const __amdgpu_buffer_rsrc_t rx = __builtin_amdgcn_make_buffer_rsrc((void*)(x + (size_t)b * 92160), 0, 92160, 0x00020000);
  const __amdgpu_buffer_rsrc_t rk = __builtin_amdgcn_make_buffer_rsrc((void*)kp, 0, 262144, 0x00020000);
  const __amdgpu_buffer_rsrc_t rv = __builtin_amdgcn_make_buffer_rsrc((void*)vp, 0, 262144, 0x00020000);
  const __amdgpu_buffer_rsrc_t rq = __builtin_amdgcn_make_buffer_rsrc((void*)wq, 0, 576 * 8192, 0x00020000);
  const __amdgpu_buffer_rsrc_t ro = __builtin_amdgcn_make_buffer_rsrc((void*)(wo + (size_t)kvh * 256 * 1152), 0, 256 * 1152, 0x00020000);
  const unsigned lane = lane_id();
  const unsigned cb = lane >> 5, row = (lane >> 2) & 7, c4 = lane & 3;
  const unsigned sk = 16 * (c4 ^ swk(2 * (j & 1) + (row >> 2))), sv = 16 * (c4 ^ (2 * (row >> 2)));
  const unsigned vk = (8 * j + row) * 256 + cb * 64 + sk;
  const unsigned vv = (8 * j + row) * 256 + cb * 64 + sv;
  const unsigned vq = (8 * j + row) * 8192 + kvh * 512 + cb * 64 + 16 * (c4 ^ (2 * (j & 1)));
  const unsigned vx = (8 * j + row) * 1152 + cb * 64 + sk;
  const unsigned vx3 = (64 + 8 * (j >> 1) + row) * 1152 + cb * 64 + 16 * (c4 ^ swk(2 * (j >> 1) + (row >> 2)));
  const bool x3on = (row >> 2) == (j & 1);
  const unsigned vo = (8 * j + row) * 1152 + cb * 64 + sv;
  const unsigned j1 = j * 1024, j2 = j * 2048, jh = (j >> 1) * 1024;
  const unsigned rot = KVROT;
  const unsigned g = lane >> 4, q4 = (lane >> 2) & 3;
  unsigned sa0 = QAREA + j * 512 + (g >> 1) * 2048 + (4 * (g & 1) + q4) * 64 + 32 * (g & 1) + 8 * c4, sa1 = sa0 ^ 32;
  asm volatile("s_nop 4" : "+v"(sa0), "+v"(sa1));
  bf16x8 W[3][2][8];
#define H_WAIT(n) wait_vm<n>()
#define H_TAG() do { tag = load_tag(ws, b); } while (0)
#define H_BURN(n) do { if (BURN) { bf16x8 a_[4]; for (int i_ = 0; i_ < 4; ++i_) a_[i_] = ldsr128(147456 + i_ * 1024 + lane * 16); f32x4 c_[4] = {}; \
    _Pragma("unroll 1") for (int it_ = 0; it_ < (n) * BURN / 16; ++it_) { _Pragma("unroll") for (int m_ = 0; m_ < 16; ++m_) c_[m_ & 3] = MFMA(a_[m_ & 3], a_[(m_ >> 2) & 3], c_[m_ & 3]); } \
    asm volatile("" :: "v"(c_[0]), "v"(c_[1]), "v"(c_[2]), "v"(c_[3])); } } while (0)
  unsigned tp = 0;
#define H_PACE(n) do { if (PACE) { if ((n) == 0) tp = (unsigned)__builtin_amdgcn_s_memrealtime() + PACE0; else if ((n) <= PACEN) { while ((int)((unsigned)__builtin_amdgcn_s_memrealtime() - tp) < (int)((n) * PACE)) __builtin_amdgcn_s_sleep(1); } } } while (0)
#define H_BAR() barrier()
#define H_X(c) do { constexpr unsigned d_ = XAREA + (((c) + 3) & 3) * 10240; dma_s<d_, 0>(j1, vx, rx, sx); dma_s<d_ + 4096, 32 * 1152>(j1, vx, rx, sx); \
    if (x3on) dma_s<d_ + 8192, 0>(jh, vx3, rx, sx); sx = sx == 1024 ? 0 : sx + 128; asm volatile("" : "+s"(sx)); } while (0)
#define H_WQ(i) do { constexpr unsigned d_ = WQAREA + (((i) + 2) & 3) * 16384; dma_s<d_, 0>(j1, vq, rq, sq); dma_s<d_ + 4096, 128>(j1, vq, rq, sq); dma_s<d_ + 8192, 256>(j1, vq, rq, sq); dma_s<d_ + 12288, 384>(j1, vq, rq, sq); \
    sq = sq == 17 * 262144 ? 0 : sq + 262144; asm volatile("" : "+s"(sq)); } while (0)
#define H_ITEM(k) do { constexpr unsigned d_ = RING + ((k) % 6) * 16384, s_ = ((k) >> 1) * 16384; const unsigned v_ = ((k) & 1) ? vv : vk; const __amdgpu_buffer_rsrc_t r_ = ((k) & 1) ? rv : rk; \
    dma_kv<d_, s_>(j2, v_, r_, rot); dma_kv<d_ + 1024, s_ + 128>(j2, v_, r_, rot); dma_kv<d_ + 8192, s_ + 8192>(j2, v_, r_, rot); dma_kv<d_ + 9216, s_ + 8320>(j2, v_, r_, rot); } while (0)
#define H_UNIT(u) do { constexpr unsigned s_ = 32 * ((u) % 8) * 1152 + 256 * ((u) / 8), d_ = QAREA + ((u) % 5) * 8192; dma<d_, s_>(j2, vo, ro); dma<d_ + 1024, s_ + 128>(j2, vo, ro); } while (0)
#define H_BLK(k) do { constexpr unsigned s_ = 64 * ((k) / 3) * 1152 + 768 + 128 * ((k) % 3), d_ = (k) < 3 ? SPARE + (k) * 8192 : (k) < 11 ? 32768 + ((k) - 3) * 8192 : 0; \
    dma<d_, s_>(j1, vo, ro); dma<d_ + 4096, s_ + 32 * 1152>(j1, vo, ro); } while (0)
#define H_PARK(u) do { W[(u) / 8][0][(u) % 8] = cat(ldstr(sa0 + ((u) % 5) * 8192), ldstr(sa0 + ((u) % 5) * 8192 + 4096)); W[(u) / 8][1][(u) % 8] = cat(ldstr(sa1 + ((u) % 5) * 8192), ldstr(sa1 + ((u) % 5) * 8192 + 4096)); \
    asm volatile("" : "+v"(W[(u) / 8][0][(u) % 8]), "+v"(W[(u) / 8][1][(u) % 8])); } while (0)
  const unsigned rc = ROTC;
  unsigned sx = rc * 128, sq = rc * 524288;
  const __amdgpu_buffer_rsrc_t rc_ = __builtin_amdgcn_make_buffer_rsrc((void*)(code0), 0, CODE_BYTES, 0x00020000);
#define H_CODE() do { touch(SCRIB, lane < 2 ? ((blockIdx.x >> 3) * 8 + j * 2 + lane) * 128 : 0xfffffff0u, rc_); } while (0)
#pragma unroll 1
  for (unsigned pass = 0;; ++pass) {
    HELP_Q_A;
    if (pass) barrier();
    HELP_EARLY_A;
    HELP_REST;
    if (pass || !redo_flag()) break;
    wait_vm<0>();
    barrier();
  }
  const __amdgpu_buffer_rsrc_t rp = __builtin_amdgcn_make_buffer_rsrc(ws + (size_t)(b * 16 + kvh) * PART_BYTES, 0, PART_BYTES, 0x00020000);
  const unsigned lane2 = lane_id();
  unsigned oa = QAREA + lane2 * 16;
  asm volatile("" : "+v"(oa));
  f32x4 acc[2][3][2];
  bf16x8 of[3];
  auto rdo = [&](auto nc) { constexpr int n = decltype(nc)::v, s = n & 7, pt = ORD[n >> 3]; of[n % 3] = ldsr128(oa + (((s >> 2) * 5 + pt) * 4 + (s & 3)) * 1024); };
  auto put = [&](auto rc2, auto pc) { constexpr int r = decltype(rc2)::v, p = decltype(pc)::v; const f32x4 a = acc[r & 1][p][0], c = acc[r & 1][p][1];
    const u32x4 v = {cvtpk(a[0], a[1]), cvtpk(a[2], a[3]), cvtpk(c[0], c[1]), cvtpk(c[2], c[3])};
    store_wt(v, rp, lane2 * 16, (r * 12 + j * 3 + p) * 1024); };
  rdo(IC<0>{}); rdo(IC<1>{});
  GAP();
  sfor<40>([&](auto nc) {
    constexpr int n = decltype(nc)::v, r = n >> 3, s = n & 7;
    const f32x4 zero = {0.f, 0.f, 0.f, 0.f};
    sfor<6>([&](auto mc) {
      constexpr int m = decltype(mc)::v, p = m >> 1, u = m & 1;
      acc[r & 1][p][u] = MFMA(W[p][u][s], of[n % 3], s == 0 ? zero : acc[r & 1][p][u]);
      GAP();
      if constexpr (m == 0 && n + 2 < 40) rdo(IC<(n + 2 < 40 ? n + 2 : 0)>{});
      if constexpr (m == 3 && r > 0 && s < 3) put(IC<(r > 0 ? r - 1 : 0)>{}, IC<(s < 3 ? s : 0)>{});
      if constexpr (m == 3 && r >= SIG0 + 2 && s == 3) { wait_vm<3>(); signal(ws, b, kvh, 4 + (r - 2) * 4 + j, tag); }
      if constexpr (m == 4 && n == BO3_AT) barrier();
      if constexpr (m == 3 && n == BO2_AT) { wait_vm<0>(); barrier(); }
      GAP();
    });
  });
  sfor<3>([&](auto pc) { put(IC<4>{}, pc); });
  if constexpr (BO3_AT == 40) barrier();
  wait_vm<3>(); signal(ws, b, kvh, 4 + 3 * 4 + j, tag);
  wait_vm<1>(); signal(ws, b, kvh, 4 + 4 * 4 + j, tag);
}

__global__ __launch_bounds__(512, 2) void gqa_main(const char* __restrict__ x, const char* __restrict__ kc, const char* __restrict__ vc, const char* __restrict__ wq, const char* __restrict__ wo, char* ws, char* out) {
  extern __shared__ __attribute__((aligned(16))) char smem[];
  unsigned long long code0; asm volatile("s_getpc_b64 %0" : "=s"(code0));
  code0 &= ~255ull;
  const unsigned w = rfl(threadIdx.x >> 6);
  const unsigned wg = blockIdx.x, kvh = 2 * (wg & 7) + ((wg >> 3) & 1), b = wg >> 4;
  const char* kp = kc + (size_t)KVSEL * 262144;
  const char* vp = vc + (size_t)KVSEL * 262144;
  unsigned long long* stamps = nullptr; unsigned long long* cst = nullptr;
  if (STAG) { const unsigned long long t_ = __builtin_amdgcn_s_memrealtime(); while (__builtin_amdgcn_s_memrealtime() - t_ < (wg >> 4) * STAG / 16) __builtin_amdgcn_s_sleep(1); }
  if (w >= 4) helper_wave(w - 4, b, kvh, x, kp, vp, wq, wo, ws, code0, stamps);
  else compute_wave(w, b, kvh, kp, ws, out, load_tag(ws, b), cst);
}

extern "C" void kernel_launch(void* const* d_in, const int* in_sizes, int n_in, void* d_out, int out_size, void* d_ws, size_t ws_size, hipStream_t stream) {
  (void)hipFuncSetAttribute((const void*)gqa_main, hipFuncAttributeMaxDynamicSharedMemorySize, (int)LDS_BYTES);
  gqa_main<<<256, 512, LDS_BYTES, stream>>>((const char*)d_in[0], (const char*)d_in[1], (const char*)d_in[2], (const char*)d_in[3], (const char*)d_in[4], (char*)d_ws, (char*)d_out);
}
```
